```python
import torch

def _softmax(x, axis=-1):
    return torch.softmax(x, dim=axis)
def _gelu(x):
    return torch.nn.functional.gelu(x, approximate="tanh")
def _silu(x):
    return torch.nn.functional.silu(x)
def _sigmoid(x):
    return torch.sigmoid(x)
def _layernorm(x, axis=-1, eps=1e-5):
    m = x.mean(dim=axis, keepdim=True)
    v = x.var(dim=axis, keepdim=True, unbiased=False)
    return (x - m) / torch.sqrt(v + eps)
def _scatter_add(tgt, idx, src, axis=0):
    return tgt.clone().index_add_(axis, idx, src)
def _segment_sum(src, seg, n):
    out = torch.zeros((n,) + src.shape[1:], dtype=src.dtype)
    return out.index_add_(0, seg, src)
def _pad(x, pw):
    flat = tuple(v for pair in reversed(pw) for v in pair)
    return torch.nn.functional.pad(x, flat)

kernel_name = 'standalone__weight_only_int_gptq__prompt'

def setup_inputs():

    _w = (lambda a: (lambda t: t.to(torch.bfloat16) if t.is_floating_point() else t)(torch.as_tensor(a)))
    B, S, D, F = 1, 2048, 576, 4608
    bits = max(2, min(4, 4))
    pf = 32 // bits
    G = -1 if -1 > 0 else D
    G = max(pf, (G // pf) * pf)
    D = max(G, (D // G) * G)
    ng = D // G
    qmax = 1 << bits
    qw = torch.randint(0, qmax, (D, F)).to(torch.int64)
    packed = torch.zeros((D // pf, F), dtype=torch.int64)
    for i in range(pf):
        packed |= (qw[i::pf] << (i * bits))
    packed = packed.to(torch.int32)
    return {
        "x": _w((lambda *s: torch.randn(*s, dtype=torch.bfloat16) * 0.05)(B, S, D)),
        "qw_packed": _w(packed),
        "scales": _w(torch.abs((lambda *s: torch.randn(*s, dtype=torch.bfloat16) * 0.05)(ng, F)).to(float) * 0.1 + 0.01),
        "zeros": _w(torch.randint(0, qmax, (ng, F)).to(float)),
        "bits": _w((lambda _x: torch.as_tensor(_x, dtype=torch.int32))(bits)),
    }

def reference(x, qw_packed, scales, zeros, bits):
    b = int(bits)
    pf = 32 // b
    mask = (1 << b) - 1
    P, F = qw_packed.shape
    D = P * pf
    ng = scales.shape[0]
    G = D // ng
    shifts = (lambda x, dt: x.to(dt))(torch.arange(pf) * b, qw_packed.dtype)
    qw = ((qw_packed[:, None, :] >> shifts[None, :, None]) & mask).reshape(D, F)
    s = (lambda x, n, axis: torch.repeat_interleave(x, n, dim=axis))(scales, G, axis=0)
    z = (lambda x, n, axis: torch.repeat_interleave(x, n, dim=axis))(zeros, G, axis=0)
    w = ((lambda x, dt: x.to(dt))(qw, x.dtype) - z) * s
    return torch.einsum("bsd,df->bsf", x, w)
```

```cpp
#include <hip/hip_runtime.h>
#include <hip/hip_bf16.h>
#include <stdint.h>
typedef __bf16 bf16x8 __attribute__((ext_vector_type(8)));
typedef float f32x4 __attribute__((ext_vector_type(4)));
typedef float f32x2 __attribute__((ext_vector_type(2)));
typedef __bf16 bf16x2 __attribute__((ext_vector_type(2)));
typedef unsigned u32x4 __attribute__((ext_vector_type(4)));
constexpr int M = 2048, K = 576, N = 4608;

__device__ __forceinline__ unsigned cvtpk(float lo, float hi) {
  f32x2 v = {lo, hi}; bf16x2 b = __builtin_convertvector(v, bf16x2); return __builtin_bit_cast(unsigned, b);
}
__device__ __forceinline__ float bf2f(unsigned short h) { return __uint_as_float((unsigned)h << 16); }

__global__ __launch_bounds__(256) void k0(const unsigned short* __restrict__ x, const unsigned* __restrict__ qw,
                                          const unsigned short* __restrict__ sc, const unsigned short* __restrict__ zr,
                                          unsigned short* __restrict__ out) {
  int lane = threadIdx.x & 63, w = threadIdx.x >> 6;
  int bn = blockIdx.x % (N / 128), bm = blockIdx.x / (N / 128);
  int c = lane & 15, g = lane >> 4;
  int col0 = bn * 128 + w * 32 + 2 * c;
  int row0 = bm * 64;
  float s0 = bf2f(sc[col0]), s1 = bf2f(sc[col0 + 1]), z0 = bf2f(zr[col0]), z1 = bf2f(zr[col0 + 1]);
  f32x4 acc[4][2] = {};
  for (int s = 0; s < 18; ++s) {
    unsigned w0 = qw[(4 * s + g) * N + col0], w1 = qw[(4 * s + g) * N + col0 + 1];
    u32x4 b0, b1;
#pragma unroll
    for (int i = 0; i < 4; ++i) {
      float a = (float)((w0 >> (8 * i)) & 15), b = (float)((w0 >> (8 * i + 4)) & 15);
      b0[i] = cvtpk((float)(__bf16)(a - z0) * s0, (float)(__bf16)(b - z0) * s0);
      a = (float)((w1 >> (8 * i)) & 15), b = (float)((w1 >> (8 * i + 4)) & 15);
      b1[i] = cvtpk((float)(__bf16)(a - z1) * s1, (float)(__bf16)(b - z1) * s1);
    }
#pragma unroll
    for (int m = 0; m < 4; ++m) {
      bf16x8 a = *(const bf16x8*)(x + (row0 + m * 16 + c) * K + s * 32 + g * 8);
      acc[m][0] = __builtin_amdgcn_mfma_f32_16x16x32_bf16(a, __builtin_bit_cast(bf16x8, b0), acc[m][0], 0, 0, 0);
      acc[m][1] = __builtin_amdgcn_mfma_f32_16x16x32_bf16(a, __builtin_bit_cast(bf16x8, b1), acc[m][1], 0, 0, 0);
    }
  }
#pragma unroll
  for (int m = 0; m < 4; ++m)
#pragma unroll
    for (int r = 0; r < 4; ++r)
      *(unsigned*)(out + (row0 + m * 16 + g * 4 + r) * N + col0) = cvtpk(acc[m][0][r], acc[m][1][r]);
}

extern "C" void kernel_launch(void* const* d_in, const int* in_sizes, int n_in, void* d_out, int out_size, void* d_ws,
                              size_t ws_size, hipStream_t stream) {
  k0<<<(M / 64) * (N / 128), 256, 0, stream>>>((const unsigned short*)d_in[0], (const unsigned*)d_in[1],
                                              (const unsigned short*)d_in[2], (const unsigned short*)d_in[3],
                                              (unsigned short*)d_out);
}
```

```cpp
#include <hip/hip_runtime.h>
#include <hip/hip_bf16.h>
#include <stdint.h>
typedef __bf16 bf16x8 __attribute__((ext_vector_type(8)));
typedef float f32x4 __attribute__((ext_vector_type(4)));
typedef float f32x2 __attribute__((ext_vector_type(2)));
typedef __bf16 bf16x2 __attribute__((ext_vector_type(2)));
typedef unsigned u32x4 __attribute__((ext_vector_type(4)));
constexpr int M = 2048, K = 576, N = 4608;

__device__ __forceinline__ unsigned cvtpk(float lo, float hi) {
  f32x2 v = {lo, hi}; bf16x2 b = __builtin_convertvector(v, bf16x2); return __builtin_bit_cast(unsigned, b);
}
__device__ __forceinline__ float bf2f(unsigned short h) { return __uint_as_float((unsigned)h << 16); }

__global__ __launch_bounds__(64) void k0(const unsigned short* __restrict__ x, const unsigned* __restrict__ qw,
                                          const unsigned short* __restrict__ sc, const unsigned short* __restrict__ zr,
                                          unsigned short* __restrict__ out) {
  unsigned long long t0 = __builtin_amdgcn_s_memrealtime();
  int lane = threadIdx.x & 63;
  for (int tile = blockIdx.x * 1 + (threadIdx.x >> 6); tile < 4608; tile += 256) {
  int w = tile & 3; int bn = (tile >> 2) % (N / 128), bm = (tile >> 2) / (N / 128);
  int c = lane & 15, g = lane >> 4;
  int col0 = bn * 128 + w * 32 + 2 * c;
  int row0 = bm * 64;
  float s0 = bf2f(sc[col0]), s1 = bf2f(sc[col0 + 1]), z0 = bf2f(zr[col0]), z1 = bf2f(zr[col0 + 1]);
  f32x4 acc[4][2] = {};
  for (int s = 0; s < 18; ++s) {
    unsigned w0 = qw[(4 * s + g) * N + col0], w1 = qw[(4 * s + g) * N + col0 + 1];
    u32x4 b0, b1;
#pragma unroll
    for (int i = 0; i < 4; ++i) {
      float a = (float)((w0 >> (8 * i)) & 15), b = (float)((w0 >> (8 * i + 4)) & 15);
      b0[i] = cvtpk((float)(__bf16)(a - z0) * s0, (float)(__bf16)(b - z0) * s0);
      a = (float)((w1 >> (8 * i)) & 15), b = (float)((w1 >> (8 * i + 4)) & 15);
      b1[i] = cvtpk((float)(__bf16)(a - z1) * s1, (float)(__bf16)(b - z1) * s1);
    }
#pragma unroll
    for (int m = 0; m < 4; ++m) {
      bf16x8 a = *(const bf16x8*)(x + (row0 + m * 16 + c) * K + s * 32 + g * 8);
      acc[m][0] = __builtin_amdgcn_mfma_f32_16x16x32_bf16(a, __builtin_bit_cast(bf16x8, b0), acc[m][0], 0, 0, 0);
      acc[m][1] = __builtin_amdgcn_mfma_f32_16x16x32_bf16(a, __builtin_bit_cast(bf16x8, b1), acc[m][1], 0, 0, 0);
    }
  }
#pragma unroll
  for (int m = 0; m < 4; ++m)
#pragma unroll
    for (int r = 0; r < 4; ++r)
      *(unsigned*)(out + (row0 + m * 16 + g * 4 + r) * N + col0) = cvtpk(acc[m][0][r], acc[m][1][r]);
  }
  asm volatile("s_waitcnt vmcnt(0)");
  while (__builtin_amdgcn_s_memrealtime() - t0 < 20000) {}
}

extern "C" void kernel_launch(void* const* d_in, const int* in_sizes, int n_in, void* d_out, int out_size, void* d_ws,
                              size_t ws_size, hipStream_t stream) {
  k0<<<256, 64, 0, stream>>>((const unsigned short*)d_in[0], (const unsigned*)d_in[1],
                                              (const unsigned short*)d_in[2], (const unsigned short*)d_in[3],
                                              (unsigned short*)d_out);
}
```

```cpp
#include <hip/hip_runtime.h>
#include <stdint.h>

__global__ __launch_bounds__(512) void gptq_gemm(const void* x, const void* qw, const void* sc, const void* zr, void* out) {
  __shared__ __attribute__((aligned(1024))) char lds[160000];
  auto ka = __builtin_amdgcn_kernarg_segment_ptr();
  int wave = __builtin_amdgcn_readfirstlane((int)(threadIdx.x >> 6));
  int bid = blockIdx.x;
  unsigned lbase = (unsigned)(uintptr_t)lds;
  asm volatile(
    "s_load_dwordx2 s[16:17], %0, 0x0\n"
    "s_load_dwordx2 s[20:21], %0, 0x8\n"
    "s_load_dwordx2 s[24:25], %0, 0x10\n"
    "s_load_dwordx2 s[28:29], %0, 0x18\n"
    "s_load_dwordx2 s[36:37], %0, 0x20\n"
    "v_mbcnt_lo_u32_b32 v243, -1, 0\n"
    "v_mbcnt_hi_u32_b32 v243, -1, v243\n"
    "s_getpc_b64 s[12:13]\n.Lpc0_%=:\n"
    "s_mov_b32 s14, .Lend_%=-.Lpc0_%=\n"
    "s_mov_b32 s15, 0x20000\n"
    "s_andn2_b32 s44, %1, 7\n"
    "s_add_u32 s44, s44, %2\n"
    "s_and_b32 s44, s44, 63\n"
    "s_mul_i32 s44, s44, 256\n"
    "v_lshlrev_b32 v255, 7, v243\n"
    "s_mov_b64 exec, 0x3\n"
    "buffer_load_dword v136, v255, s[12:15], s44 offen\n"
    "s_mov_b64 exec, -1\n"
    "s_mov_b32 s18, 0x240000\n"
    "s_mov_b32 s19, 0x20000\n"
    "s_mov_b32 s22, 0x144000\n"
    "s_mov_b32 s23, 0x20000\n"
    "s_lshl_b32 s44, %1, 2\n"
    "s_lshr_b32 s48, 0x64317520, s44\n"
    "s_bfe_u32 s45, s48, 0x20001\n"
    "s_bfe_u32 s44, %1, 0x20003\n"
    "s_lshl2_add_u32 s45, s45, s44\n"
    "s_and_b32 s48, s48, 1\n"
    "s_lshr_b32 s46, %1, 5\n"
    "s_lshl3_add_u32 s48, s48, s46\n"
    "s_xor_b32 s53, s44, s46\n"
    "s_and_b32 s53, s53, 1\n"
    "s_mul_i32 s53, s53, 5\n"
    "s_bfe_u32 s54, %1, 0x10006\n"
    "s_mov_b32 s64, 0x12000\n"
    "s_mul_i32 s43, %2, 9216\n"
    "v_and_b32 v253, 15, v243\n"
    "v_lshrrev_b32 v254, 4, v243\n"
    "s_lshl1_add_u32 s44, s45, s54\n"
    "s_lshl3_add_u32 s44, s44, %2\n"
    "s_mul_i32 s44, s44, 9216\n"
    "s_lshl_b32 s46, s53, 7\n"
    "s_add_u32 s44, s44, s46\n"
    "v_lshrrev_b32 v252, 3, v243\n"
    "v_mul_u32_u24 v250, 1152, v252\n"
    "v_and_b32 v255, 7, v243\n"
    "v_lshlrev_b32 v242, 1, v254\n"
    "v_xor_b32 v242, v255, v242\n"
    "v_lshl_add_u32 v250, v242, 4, v250\n"
    "v_add_u32 v250, s44, v250\n"
    "s_mul_i32 s52, s48, 1152\n"
    "s_lshl_b32 s46, %2, 7\n"
    "s_add_u32 s47, s52, s46\n"
    "s_mul_i32 s46, s53, 0x24000\n"
    "s_add_u32 s44, s47, s46\n"
    "v_mul_u32_u24 v251, 18432, v254\n"
    "v_lshl_add_u32 v251, v253, 3, v251\n"
    "v_add_u32 v251, s44, v251\n"
    "s_add_u32 m0, s43, 0\n"
    "s_waitcnt lgkmcnt(0)\n"
    "buffer_load_dwordx4 v250, s[16:19], 0 offen offset:0 lds\n"
    "buffer_load_dwordx2 v[6:7], v251, s[20:23], 0 offen\n"
    "buffer_load_dwordx2 v[14:15], v251, s[20:23], s64 offen\n"
    "s_mov_b32 s65, 0x24000\n"
    "s_mov_b32 s66, 0x36000\n"
    "s_mov_b32 s67, 0x48000\n"
    "s_mov_b32 s68, 0x5a000\n"
    "s_mov_b32 s69, 0x6c000\n"
    "s_mov_b32 s70, 0x7e000\n"
    "s_mov_b32 s71, 0x90000\n"
    "s_mov_b32 s72, 0xa2000\n"
    "s_mov_b32 s26, 0x2400\n"
    "s_mov_b32 s27, 0x20000\n"
    "s_mov_b32 s38, 0x1200000\n"
    "s_mov_b32 s39, 0x20000\n"
    "s_mov_b32 s40, 0x000f000f\n"
    "s_mov_b32 s41, 0x00040000\n"
    "s_mov_b32 s42, 0x000c0008\n"
    "s_mov_b32 s49, 0xffff0000\n"
    "v_mul_u32_u24 v252, 18432, v252\n"
    "v_lshl_add_u32 v255, v255, 4, v252\n"
    "s_add_u32 s44, s52, 1024\n"
    "v_add_u32 v255, s44, v255\n"
    "s_movk_i32 s73, 512\n"
    "s_cmp_eq_u32 s53, 5\n"
    "s_cmovk_i32 s73, -640\n"
    "s_mul_i32 s46, s54, 0x24000\n"
    "s_sub_u32 s74, s64, s46\n"
    "s_lshl_b32 s76, s74, 3\n"
    "s_sub_u32 s74, s74, s73\n"
    "s_add_u32 s77, s53, %2\n"
    "s_sub_u32 s46, s77, 9\n"
    "s_cselect_b32 s77, s77, s46\n"
    "s_sub_u32 s78, s53, 1\n"
    "s_cselect_b32 s78, 8, s78\n"
    "s_mul_i32 s77, s77, 0x24000\n"
    "s_mul_i32 s78, s78, 0x24000\n"
    "s_lshl_b32 s79, %2, 10\n"
    "s_add_u32 s79, s79, 0x24d00\n"
    "s_lshl_b32 s44, %2, 3\n"
    "s_lshl_b64 s[58:59], 0xff, s44\n"
    "s_lshr_b32 s52, s52, 1\n"
    "s_lshr_b32 s47, s47, 1\n"
    "s_lshl1_add_u32 s44, s45, s54\n"
    "s_mul_i32 s44, s44, 0x90000\n"
    "s_add_u32 s44, s44, s47\n"
    "v_mul_u32_u24 v248, 9216, v253\n"
    "v_lshl_add_u32 v248, v254, 4, v248\n"
    "v_add_u32 v248, s44, v248\n"
    "s_and_b32 s44, %2, 3\n"
    "s_mul_i32 s51, s44, 0x24000\n"
    "s_lshl_b32 s46, %2, 6\n"
    "s_sub_u32 s51, s51, s46\n"
    "s_add_u32 s51, s51, 512\n"
    "s_cmp_lt_u32 %2, 4\n"
    "s_cselect_b32 s46, s76, 0\n"
    "s_sub_u32 s51, s51, s46\n"
    "s_lshr_b32 s46, %2, 2\n"
    "s_lshl4_add_u32 s55, s46, s44\n"
    "s_lshl_b32 s55, s55, 10\n"
    "s_lshl_b32 s56, %2, 12\n"
    "v_mov_b32 v224, 0x43004300\n"
    "v_mov_b32 v225, 0x43004300\n"
    "v_mov_b32 v226, 0x43004300\n"
    "v_mov_b32 v227, 0x43004300\n"
    "v_mov_b32 v228, 0\n"
    "v_mov_b32 v229, 0\n"
    "v_mov_b32 v230, 0\n"
    "v_mov_b32 v231, 0\n"
    "v_and_b32 v241, 1, v243\n"
    "v_mul_u32_u24 v241, 0xaaaa, v241\n"
    "v_add_u32 v241, 0x4444, v241\n"
    "v_lshl_or_b32 v241, v241, 16, v241\n"
    "v_lshlrev_b32 v252, 4, v243\n"
    "s_lshr_b32 s44, %2, 1\n"
    "s_mul_i32 s44, s44, 144\n"
    "s_and_b32 s47, %2, 1\n"
    "v_lshl_add_u32 v234, v243, 2, s44\n"
    "v_and_b32 v235, -16, v234\n"
    "s_mul_i32 s44, s47, 1408\n"
    "s_add_u32 s44, s44, 0x24200\n"
    "v_add3_u32 v235, v235, v234, s44\n"
    "s_cmp_eq_u32 s47, 1\n"
    "s_cselect_b32 s24, s28, s24\n"
    "s_cselect_b32 s25, s29, s25\n"
    "s_cselect_b32 s50, 0xbc000000, 1.0\n"
    "s_cselect_b32 s57, -1.0, 0\n"
    "v_mov_b32 v236, s50\n"
    "v_lshrrev_b32 v247, 3, v253\n"
    "v_mul_u32_u24 v244, 9216, v247\n"
    "v_and_b32 v242, 7, v253\n"
    "v_lshl_add_u32 v244, v242, 7, v244\n"
    "v_and_b32 v245, 6, v253\n"
    "v_xor_b32 v245, v245, v254\n"
    "v_lshl_add_u32 v244, v245, 4, v244\n"
    "v_xor_b32 v245, 64, v244\n"
    "s_and_b32 s44, %2, 2\n"
    "s_lshl_b32 s44, s44, 5\n"
    "s_mul_i32 s46, s46, 0x12000\n"
    "v_xor_b32 v240, s44, v244\n"
    "v_add_u32 v240, s46, v240\n"
    "v_lshl_add_u32 v247, v247, 2, v254\n"
    "v_and_b32 v243, 2, v253\n"
    "v_xor_b32 v247, v247, v243\n"
    "v_lshlrev_b32 v247, 4, v247\n"
    "v_lshl_add_u32 v247, v242, 7, v247\n"
    "v_add_u32 v247, s43, v247\n"
    "s_lshl_b32 s44, s44, 3\n"
    "s_lshl2_add_u32 s44, s47, s44\n"
    "s_add_u32 s44, s44, 0x24d00\n"
    "v_lshrrev_b32 v246, 1, v252\n"
    "v_add_u32 v246, s44, v246\n"
    "v_lshlrev_b32 v249, 2, v253\n"
    "v_add_u32 v249, 0x24000, v249\n"
    "v_lshlrev_b32 v253, 5, v254\n"
    "v_add_u32 v253, 0x24200, v253\n"
    "s_lshl_b32 s44, %2, 5\n"
    "s_add_u32 s44, s44, 0x24000\n"
    "v_lshl_add_u32 v254, v242, 2, s44\n"
    "s_add_u32 m0, s43, 896\n"
    "s_nop 0\n"
    "buffer_load_dwordx4 v250, s[16:19], 0 offen offset:128 lds\n"
    "buffer_load_dwordx2 v[22:23], v251, s[20:23], s65 offen\n"
    "buffer_load_dwordx2 v[30:31], v251, s[20:23], s66 offen\n"
    "s_barrier\n"
    "s_add_u32 m0, s43, 1792\n"
    "s_nop 0\n"
    "buffer_load_dwordx4 v250, s[16:19], 0 offen offset:256 lds\n"
    "buffer_load_dwordx2 v[38:39], v251, s[20:23], s67 offen\n"
    "buffer_load_dwordx2 v[46:47], v251, s[20:23], s68 offen\n"
    "s_waitcnt vmcnt(7)\n"
    "v_pk_lshrrev_b16 v0, s41, v6 op_sel:[0,0] op_sel_hi:[1,0]\n"
    "v_pk_lshrrev_b16 v1, s42, v6 op_sel:[0,0] op_sel_hi:[1,0]\n"
    "v_pk_lshrrev_b16 v2, s41, v6 op_sel:[0,1] op_sel_hi:[1,1]\n"
    "v_pk_lshrrev_b16 v3, s42, v6 op_sel:[0,1] op_sel_hi:[1,1]\n"
    "v_and_or_b32 v0, v0, s40, v224\n"
    "v_and_or_b32 v1, v1, s40, v224\n"
    "v_and_or_b32 v2, v2, s40, v224\n"
    "v_and_or_b32 v3, v3, s40, v224\n"
    "v_pk_lshrrev_b16 v4, s41, v7 op_sel:[0,0] op_sel_hi:[1,0]\n"
    "v_pk_lshrrev_b16 v5, s42, v7 op_sel:[0,0] op_sel_hi:[1,0]\n"
    "v_pk_lshrrev_b16 v6, s41, v7 op_sel:[0,1] op_sel_hi:[1,1]\n"
    "v_pk_lshrrev_b16 v7, s42, v7 op_sel:[0,1] op_sel_hi:[1,1]\n"
    "v_and_or_b32 v4, v4, s40, v224\n"
    "v_and_or_b32 v5, v5, s40, v224\n"
    "v_and_or_b32 v6, v6, s40, v224\n"
    "v_and_or_b32 v7, v7, s40, v224\n"
    "s_barrier\n"
    "ds_read_b128 v[176:179], v244 offset:0\n"
    "ds_read_b128 v[180:183], v244 offset:18432\n"
    "ds_read_b128 v[184:187], v244 offset:36864\n"
    "ds_read_b128 v[188:191], v244 offset:55296\n"
    "ds_read_b128 v[192:195], v245 offset:0\n"
    "s_waitcnt lgkmcnt(4)\n"
    "v_mfma_f32_16x16x32_bf16 v[144:147], v[0:3], v[176:179], 0\n"
    "ds_read_b128 v[196:199], v245 offset:18432\n"
    "s_setprio 3\n"
    "s_waitcnt vmcnt(6)\n"
    "v_pk_lshrrev_b16 v8, s41, v14 op_sel:[0,0] op_sel_hi:[1,0]\n"
    "v_pk_lshrrev_b16 v9, s42, v14 op_sel:[0,0] op_sel_hi:[1,0]\n"
    "v_mfma_f32_16x16x32_bf16 v[148:151], v[4:7], v[176:179], 0\n"
    "s_waitcnt lgkmcnt(3)\n"
    "s_add_u32 m0, s43, 2688\n"
    "v_pk_lshrrev_b16 v10, s41, v14 op_sel:[0,1] op_sel_hi:[1,1]\n"
    "v_pk_lshrrev_b16 v11, s42, v14 op_sel:[0,1] op_sel_hi:[1,1]\n"
    "v_mfma_f32_16x16x32_bf16 v[156:159], v[4:7], v[180:183], 0\n"
    "ds_read_b128 v[176:179], v245 offset:36864\n"
    "s_setprio 1\n"
    "v_and_or_b32 v8, v8, s40, v224\n"
    "v_and_or_b32 v9, v9, s40, v224\n"
    "v_mfma_f32_16x16x32_bf16 v[152:155], v[0:3], v[180:183], 0\n"
    "buffer_load_dwordx4 v250, s[16:19], 0 offen offset:384 lds\n"
    "v_and_or_b32 v10, v10, s40, v224\n"
    "v_and_or_b32 v11, v11, s40, v224\n"
    "v_mfma_f32_16x16x32_bf16 v[160:163], v[0:3], v[184:187], 0\n"
    "ds_read_b128 v[180:183], v245 offset:55296\n"
    "s_setprio 0\n"
    "v_pk_lshrrev_b16 v12, s41, v15 op_sel:[0,0] op_sel_hi:[1,0]\n"
    "v_pk_lshrrev_b16 v13, s42, v15 op_sel:[0,0] op_sel_hi:[1,0]\n"
    "v_mfma_f32_16x16x32_bf16 v[164:167], v[4:7], v[184:187], 0\n"
    "s_waitcnt lgkmcnt(3)\n"
    "v_pk_lshrrev_b16 v14, s41, v15 op_sel:[0,1] op_sel_hi:[1,1]\n"
    "v_pk_lshrrev_b16 v15, s42, v15 op_sel:[0,1] op_sel_hi:[1,1]\n"
    "v_mfma_f32_16x16x32_bf16 v[172:175], v[4:7], v[188:191], 0\n"
    "s_waitcnt vmcnt(6)\n"
    "s_barrier\n"
    "ds_read_b128 v[184:187], v244 offset:1024\n"
    "s_setprio 3\n"
    "v_and_or_b32 v12, v12, s40, v224\n"
    "v_and_or_b32 v13, v13, s40, v224\n"
    "v_mfma_f32_16x16x32_bf16 v[168:171], v[0:3], v[188:191], 0\n"
    "buffer_load_dwordx2 v[54:55], v251, s[20:23], s69 offen\n"
    "v_and_or_b32 v14, v14, s40, v224\n"
    "v_and_or_b32 v15, v15, s40, v224\n"
    "v_mfma_f32_16x16x32_bf16 v[144:147], v[8:11], v[192:195], v[144:147]\n"
    "ds_read_b128 v[188:191], v244 offset:19456\n"
    "s_waitcnt vmcnt(6)\n"
    "v_pk_lshrrev_b16 v16, s41, v22 op_sel:[0,0] op_sel_hi:[1,0]\n"
    "v_pk_lshrrev_b16 v17, s42, v22 op_sel:[0,0] op_sel_hi:[1,0]\n"
    "v_mfma_f32_16x16x32_bf16 v[148:151], v[12:15], v[192:195], v[148:151]\n"
    "s_waitcnt lgkmcnt(3)\n"
    "v_pk_lshrrev_b16 v18, s41, v22 op_sel:[0,1] op_sel_hi:[1,1]\n"
    "v_pk_lshrrev_b16 v19, s42, v22 op_sel:[0,1] op_sel_hi:[1,1]\n"
    "v_mfma_f32_16x16x32_bf16 v[156:159], v[12:15], v[196:199], v[156:159]\n"
    "ds_read_b128 v[192:195], v244 offset:37888\n"
    "s_setprio 2\n"
    "v_and_or_b32 v16, v16, s40, v224\n"
    "v_and_or_b32 v17, v17, s40, v224\n"
    "v_mfma_f32_16x16x32_bf16 v[152:155], v[8:11], v[196:199], v[152:155]\n"
    "buffer_load_dwordx2 v[62:63], v251, s[20:23], s70 offen\n"
    "v_add_u32 v250, s73, v250\n"
    "s_add_u32 m0, s43, 4096\n"
    "v_and_or_b32 v18, v18, s40, v224\n"
    "v_mfma_f32_16x16x32_bf16 v[160:163], v[8:11], v[176:179], v[160:163]\n"
    "ds_read_b128 v[196:199], v244 offset:56320\n"
    "v_and_or_b32 v19, v19, s40, v224\n"
    "v_pk_lshrrev_b16 v20, s41, v23 op_sel:[0,0] op_sel_hi:[1,0]\n"
    "v_mfma_f32_16x16x32_bf16 v[164:167], v[12:15], v[176:179], v[164:167]\n"
    "s_waitcnt lgkmcnt(3)\n"
    "v_pk_lshrrev_b16 v21, s42, v23 op_sel:[0,0] op_sel_hi:[1,0]\n"
    "v_pk_lshrrev_b16 v22, s41, v23 op_sel:[0,1] op_sel_hi:[1,1]\n"
    "v_mfma_f32_16x16x32_bf16 v[172:175], v[12:15], v[180:183], v[172:175]\n"
    "ds_read_b128 v[176:179], v245 offset:1024\n"
    "s_setprio 1\n"
    "v_pk_lshrrev_b16 v23, s42, v23 op_sel:[0,1] op_sel_hi:[1,1]\n"
    "v_and_or_b32 v20, v20, s40, v224\n"
    "v_mfma_f32_16x16x32_bf16 v[168:171], v[8:11], v[180:183], v[168:171]\n"
    "buffer_load_dwordx4 v250, s[16:19], 0 offen offset:0 lds\n"
    "s_mul_i32 s75, s73, 1152\n"
    "v_add_u32 v251, s75, v251\n"
    "v_and_or_b32 v21, v21, s40, v224\n"
    "v_and_or_b32 v22, v22, s40, v224\n"
    "v_and_or_b32 v23, v23, s40, v224\n"
    "v_mfma_f32_16x16x32_bf16 v[144:147], v[16:19], v[184:187], v[144:147]\n"
    "ds_read_b128 v[180:183], v245 offset:19456\n"
    "s_waitcnt vmcnt(7)\n"
    "v_pk_lshrrev_b16 v24, s41, v30 op_sel:[0,0] op_sel_hi:[1,0]\n"
    "v_pk_lshrrev_b16 v25, s42, v30 op_sel:[0,0] op_sel_hi:[1,0]\n"
    "v_mfma_f32_16x16x32_bf16 v[148:151], v[20:23], v[184:187], v[148:151]\n"
    "s_waitcnt lgkmcnt(3)\n"
    "v_pk_lshrrev_b16 v26, s41, v30 op_sel:[0,1] op_sel_hi:[1,1]\n"
    "v_pk_lshrrev_b16 v27, s42, v30 op_sel:[0,1] op_sel_hi:[1,1]\n"
    "v_mfma_f32_16x16x32_bf16 v[156:159], v[20:23], v[188:191], v[156:159]\n"
    "ds_read_b128 v[184:187], v245 offset:37888\n"
    "s_setprio 0\n"
    "v_and_or_b32 v24, v24, s40, v224\n"
    "v_and_or_b32 v25, v25, s40, v224\n"
    "v_mfma_f32_16x16x32_bf16 v[152:155], v[16:19], v[188:191], v[152:155]\n"
    "buffer_load_dwordx2 v[70:71], v251, s[20:23], 0 offen\n"
    "v_and_or_b32 v26, v26, s40, v224\n"
    "v_and_or_b32 v27, v27, s40, v224\n"
    "v_mfma_f32_16x16x32_bf16 v[160:163], v[16:19], v[192:195], v[160:163]\n"
    "ds_read_b128 v[188:191], v245 offset:56320\n"
    "v_pk_lshrrev_b16 v28, s41, v31 op_sel:[0,0] op_sel_hi:[1,0]\n"
    "v_pk_lshrrev_b16 v29, s42, v31 op_sel:[0,0] op_sel_hi:[1,0]\n"
    "v_mfma_f32_16x16x32_bf16 v[164:167], v[20:23], v[192:195], v[164:167]\n"
    "s_waitcnt lgkmcnt(3)\n"
    "v_pk_lshrrev_b16 v30, s41, v31 op_sel:[0,1] op_sel_hi:[1,1]\n"
    "v_pk_lshrrev_b16 v31, s42, v31 op_sel:[0,1] op_sel_hi:[1,1]\n"
    "v_mfma_f32_16x16x32_bf16 v[172:175], v[20:23], v[196:199], v[172:175]\n"
    "s_waitcnt vmcnt(7)\n"
    "s_barrier\n"
    "ds_read_b128 v[192:195], v244 offset:2048\n"
    "s_setprio 3\n"
    "v_and_or_b32 v28, v28, s40, v224\n"
    "v_and_or_b32 v29, v29, s40, v224\n"
    "v_mfma_f32_16x16x32_bf16 v[168:171], v[16:19], v[196:199], v[168:171]\n"
    "buffer_load_dwordx2 v[78:79], v251, s[20:23], s64 offen\n"
    "v_and_or_b32 v30, v30, s40, v224\n"
    "v_and_or_b32 v31, v31, s40, v224\n"
    "v_mfma_f32_16x16x32_bf16 v[144:147], v[24:27], v[176:179], v[144:147]\n"
    "ds_read_b128 v[196:199], v244 offset:20480\n"
    "s_waitcnt vmcnt(7)\n"
    "v_pk_lshrrev_b16 v32, s41, v38 op_sel:[0,0] op_sel_hi:[1,0]\n"
    "v_pk_lshrrev_b16 v33, s42, v38 op_sel:[0,0] op_sel_hi:[1,0]\n"
    "v_mfma_f32_16x16x32_bf16 v[148:151], v[28:31], v[176:179], v[148:151]\n"
    "s_waitcnt lgkmcnt(3)\n"
    "v_pk_lshrrev_b16 v34, s41, v38 op_sel:[0,1] op_sel_hi:[1,1]\n"
    "v_pk_lshrrev_b16 v35, s42, v38 op_sel:[0,1] op_sel_hi:[1,1]\n"
    "v_mfma_f32_16x16x32_bf16 v[156:159], v[28:31], v[180:183], v[156:159]\n"
    "ds_read_b128 v[176:179], v244 offset:38912\n"
    "s_setprio 2\n"
    "v_and_or_b32 v32, v32, s40, v224\n"
    "v_and_or_b32 v33, v33, s40, v224\n"
    "v_mfma_f32_16x16x32_bf16 v[152:155], v[24:27], v[180:183], v[152:155]\n"
    "buffer_load_dword v233, v234, s[24:27], s52 offen\n"
    "s_mov_b32 m0, s79\n"
    "v_and_or_b32 v34, v34, s40, v224\n"
    "v_and_or_b32 v35, v35, s40, v224\n"
    "v_mfma_f32_16x16x32_bf16 v[160:163], v[24:27], v[184:187], v[160:163]\n"
    "ds_read_b128 v[180:183], v244 offset:57344\n"
    "v_pk_lshrrev_b16 v36, s41, v39 op_sel:[0,0] op_sel_hi:[1,0]\n"
    "v_pk_lshrrev_b16 v37, s42, v39 op_sel:[0,0] op_sel_hi:[1,0]\n"
    "v_mfma_f32_16x16x32_bf16 v[164:167], v[28:31], v[184:187], v[164:167]\n"
    "s_waitcnt lgkmcnt(3)\n"
    "v_pk_lshrrev_b16 v38, s41, v39 op_sel:[0,1] op_sel_hi:[1,1]\n"
    "v_pk_lshrrev_b16 v39, s42, v39 op_sel:[0,1] op_sel_hi:[1,1]\n"
    "v_mfma_f32_16x16x32_bf16 v[172:175], v[28:31], v[188:191], v[172:175]\n"
    "ds_read_b128 v[184:187], v245 offset:2048\n"
    "v_and_or_b32 v36, v36, s40, v224\n"
    "v_and_or_b32 v37, v37, s40, v224\n"
    "v_mfma_f32_16x16x32_bf16 v[168:171], v[24:27], v[188:191], v[168:171]\n"
    "buffer_load_dwordx4 v255, s[20:23], s77 offen lds\n"
    "s_mov_b32 m0, 0x26d00\n"
    "v_and_or_b32 v38, v38, s40, v224\n"
    "v_and_or_b32 v39, v39, s40, v224\n"
    "v_mfma_f32_16x16x32_bf16 v[144:147], v[32:35], v[192:195], v[144:147]\n"
    "ds_read_b128 v[188:191], v245 offset:20480\n"
    "s_setprio 1\n"
    "s_waitcnt vmcnt(8)\n"
    "v_pk_lshrrev_b16 v40, s41, v46 op_sel:[0,0] op_sel_hi:[1,0]\n"
    "v_pk_lshrrev_b16 v41, s42, v46 op_sel:[0,0] op_sel_hi:[1,0]\n"
    "v_mfma_f32_16x16x32_bf16 v[148:151], v[36:39], v[192:195], v[148:151]\n"
    "s_waitcnt lgkmcnt(3)\n"
    "v_pk_lshrrev_b16 v42, s41, v46 op_sel:[0,1] op_sel_hi:[1,1]\n"
    "v_pk_lshrrev_b16 v43, s42, v46 op_sel:[0,1] op_sel_hi:[1,1]\n"
    "v_mfma_f32_16x16x32_bf16 v[156:159], v[36:39], v[196:199], v[156:159]\n"
    "ds_read_b128 v[192:195], v247 offset:0\n"
    "v_and_or_b32 v40, v40, s40, v224\n"
    "v_and_or_b32 v41, v41, s40, v224\n"
    "v_mfma_f32_16x16x32_bf16 v[152:155], v[32:35], v[196:199], v[152:155]\n"
    "s_mov_b64 exec, s[58:59]\nbuffer_load_dwordx4 v255, s[20:23], s78 offen lds\ns_mov_b64 exec, -1\n"
    "s_add_u32 m0, s43, 4992\n"
    "v_and_or_b32 v42, v42, s40, v224\n"
    "v_and_or_b32 v43, v43, s40, v224\n"
    "v_mfma_f32_16x16x32_bf16 v[160:163], v[32:35], v[176:179], v[160:163]\n"
    "ds_read_b128 v[196:199], v247 offset:1024\n"
    "s_setprio 0\n"
    "v_pk_lshrrev_b16 v44, s41, v47 op_sel:[0,0] op_sel_hi:[1,0]\n"
    "v_pk_lshrrev_b16 v45, s42, v47 op_sel:[0,0] op_sel_hi:[1,0]\n"
    "v_mfma_f32_16x16x32_bf16 v[164:167], v[36:39], v[176:179], v[164:167]\n"
    "s_waitcnt lgkmcnt(3)\n"
    "v_pk_lshrrev_b16 v46, s41, v47 op_sel:[0,1] op_sel_hi:[1,1]\n"
    "v_pk_lshrrev_b16 v47, s42, v47 op_sel:[0,1] op_sel_hi:[1,1]\n"
    "v_mfma_f32_16x16x32_bf16 v[172:175], v[36:39], v[180:183], v[172:175]\n"
    "ds_read_b128 v[176:179], v245 offset:38912\n"
    "v_and_or_b32 v44, v44, s40, v224\n"
    "v_and_or_b32 v45, v45, s40, v224\n"
    "v_mfma_f32_16x16x32_bf16 v[168:171], v[32:35], v[180:183], v[168:171]\n"
    "buffer_load_dwordx4 v250, s[16:19], 0 offen offset:128 lds\n"
    "v_and_or_b32 v46, v46, s40, v224\n"
    "v_and_or_b32 v47, v47, s40, v224\n"
    "v_mfma_f32_16x16x32_bf16 v[144:147], v[40:43], v[184:187], v[144:147]\n"
    "ds_read_b128 v[180:183], v245 offset:57344\n"
    "s_waitcnt vmcnt(8)\n"
    "v_pk_lshrrev_b16 v48, s41, v54 op_sel:[0,0] op_sel_hi:[1,0]\n"
    "v_pk_lshrrev_b16 v49, s42, v54 op_sel:[0,0] op_sel_hi:[1,0]\n"
    "v_mfma_f32_16x16x32_bf16 v[148:151], v[44:47], v[184:187], v[148:151]\n"
    "s_waitcnt lgkmcnt(3)\n"
    "v_pk_lshrrev_b16 v50, s41, v54 op_sel:[0,1] op_sel_hi:[1,1]\n"
    "v_pk_lshrrev_b16 v51, s42, v54 op_sel:[0,1] op_sel_hi:[1,1]\n"
    "v_mfma_f32_16x16x32_bf16 v[156:159], v[44:47], v[188:191], v[156:159]\n"
    "s_barrier\n"
    "ds_read_b128 v[184:187], v244 offset:3072\n"
    "s_setprio 3\n"
    "v_and_or_b32 v48, v48, s40, v224\n"
    "v_and_or_b32 v49, v49, s40, v224\n"
    "v_mfma_f32_16x16x32_bf16 v[152:155], v[40:43], v[188:191], v[152:155]\n"
    "s_waitcnt lgkmcnt(3)\n"
    "ds_read_b128 v[188:191], v244 offset:21504\n"
    "buffer_load_dwordx2 v[86:87], v251, s[20:23], s65 offen\n"
    "v_and_or_b32 v50, v50, s40, v224\n"
    "v_and_or_b32 v51, v51, s40, v224\n"
    "v_smfmac_f32_16x16x64_bf16 v[228:231], v[224:227], v[192:199], v241\n"
    "s_waitcnt lgkmcnt(2)\n"
    "ds_read_b128 v[192:195], v244 offset:39936\n"
    "s_setprio 2\n"
    "v_pk_lshrrev_b16 v52, s41, v55 op_sel:[0,0] op_sel_hi:[1,0]\n"
    "v_pk_lshrrev_b16 v53, s42, v55 op_sel:[0,0] op_sel_hi:[1,0]\n"
    "v_mfma_f32_16x16x32_bf16 v[160:163], v[40:43], v[176:179], v[160:163]\n"
    "ds_read_b128 v[196:199], v244 offset:58368\n"
    "v_pk_lshrrev_b16 v54, s41, v55 op_sel:[0,1] op_sel_hi:[1,1]\n"
    "v_pk_lshrrev_b16 v55, s42, v55 op_sel:[0,1] op_sel_hi:[1,1]\n"
    "v_mfma_f32_16x16x32_bf16 v[164:167], v[44:47], v[176:179], v[164:167]\n"
    "v_and_or_b32 v52, v52, s40, v224\n"
    "v_and_or_b32 v53, v53, s40, v224\n"
    "v_mfma_f32_16x16x32_bf16 v[172:175], v[44:47], v[180:183], v[172:175]\n"
    "ds_read_b128 v[176:179], v245 offset:3072\n"
    "s_setprio 1\n"
    "v_and_or_b32 v54, v54, s40, v224\n"
    "v_and_or_b32 v55, v55, s40, v224\n"
    "v_mfma_f32_16x16x32_bf16 v[168:171], v[40:43], v[180:183], v[168:171]\n"
    "s_waitcnt lgkmcnt(3)\n"
    "buffer_load_dwordx2 v[94:95], v251, s[20:23], s66 offen\n"
    "s_add_u32 m0, s43, 5888\n"
    "v_mfma_f32_16x16x32_bf16 v[144:147], v[48:51], v[184:187], v[144:147]\n"
    "ds_read_b128 v[180:183], v245 offset:21504\n"
    "s_waitcnt vmcnt(9)\n"
    "v_pk_lshrrev_b16 v56, s41, v62 op_sel:[0,0] op_sel_hi:[1,0]\n"
    "v_pk_lshrrev_b16 v57, s42, v62 op_sel:[0,0] op_sel_hi:[1,0]\n"
    "v_mfma_f32_16x16x32_bf16 v[148:151], v[52:55], v[184:187], v[148:151]\n"
    "v_pk_lshrrev_b16 v58, s41, v62 op_sel:[0,1] op_sel_hi:[1,1]\n"
    "v_pk_lshrrev_b16 v59, s42, v62 op_sel:[0,1] op_sel_hi:[1,1]\n"
    "v_mfma_f32_16x16x32_bf16 v[156:159], v[52:55], v[188:191], v[156:159]\n"
    "ds_read_b128 v[184:187], v245 offset:39936\n"
    "s_setprio 0\n"
    "v_and_or_b32 v56, v56, s40, v224\n"
    "v_and_or_b32 v57, v57, s40, v224\n"
    "v_mfma_f32_16x16x32_bf16 v[152:155], v[48:51], v[188:191], v[152:155]\n"
    "s_waitcnt lgkmcnt(3)\n"
    "buffer_load_dwordx4 v250, s[16:19], 0 offen offset:256 lds\n"
    "v_and_or_b32 v58, v58, s40, v224\n"
    "v_and_or_b32 v59, v59, s40, v224\n"
    "v_mfma_f32_16x16x32_bf16 v[160:163], v[48:51], v[192:195], v[160:163]\n"
    "ds_read_b128 v[188:191], v245 offset:58368\n"
    "v_pk_lshrrev_b16 v60, s41, v63 op_sel:[0,0] op_sel_hi:[1,0]\n"
    "v_pk_lshrrev_b16 v61, s42, v63 op_sel:[0,0] op_sel_hi:[1,0]\n"
    "v_mfma_f32_16x16x32_bf16 v[164:167], v[52:55], v[192:195], v[164:167]\n"
    "v_pk_lshrrev_b16 v62, s41, v63 op_sel:[0,1] op_sel_hi:[1,1]\n"
    "v_pk_lshrrev_b16 v63, s42, v63 op_sel:[0,1] op_sel_hi:[1,1]\n"
    "v_mfma_f32_16x16x32_bf16 v[172:175], v[52:55], v[196:199], v[172:175]\n"
    "s_waitcnt vmcnt(9)\n"
    "s_barrier\n"
    "ds_read_b128 v[192:195], v244 offset:4096\n"
    "s_setprio 3\n"
    "v_and_or_b32 v60, v60, s40, v224\n"
    "v_and_or_b32 v61, v61, s40, v224\n"
    "v_mfma_f32_16x16x32_bf16 v[168:171], v[48:51], v[196:199], v[168:171]\n"
    "s_waitcnt lgkmcnt(3)\n"
    "buffer_load_dwordx2 v[102:103], v251, s[20:23], s67 offen\n"
    "v_and_or_b32 v62, v62, s40, v224\n"
    "v_and_or_b32 v63, v63, s40, v224\n"
    "v_mfma_f32_16x16x32_bf16 v[144:147], v[56:59], v[176:179], v[144:147]\n"
    "ds_read_b128 v[196:199], v244 offset:22528\n"
    "s_waitcnt vmcnt(9)\n"
    "v_pk_lshrrev_b16 v64, s41, v70 op_sel:[0,0] op_sel_hi:[1,0]\n"
    "v_pk_lshrrev_b16 v65, s42, v70 op_sel:[0,0] op_sel_hi:[1,0]\n"
    "v_mfma_f32_16x16x32_bf16 v[148:151], v[60:63], v[176:179], v[148:151]\n"
    "v_pk_lshrrev_b16 v66, s41, v70 op_sel:[0,1] op_sel_hi:[1,1]\n"
    "v_pk_lshrrev_b16 v67, s42, v70 op_sel:[0,1] op_sel_hi:[1,1]\n"
    "v_mfma_f32_16x16x32_bf16 v[156:159], v[60:63], v[180:183], v[156:159]\n"
    "ds_read_b128 v[176:179], v247 offset:2048\n"
    "s_setprio 2\n"
    "v_and_or_b32 v64, v64, s40, v224\n"
    "v_and_or_b32 v65, v65, s40, v224\n"
    "v_mfma_f32_16x16x32_bf16 v[152:155], v[56:59], v[180:183], v[152:155]\n"
    "s_waitcnt lgkmcnt(3)\n"
    "buffer_load_dwordx2 v[110:111], v251, s[20:23], s68 offen\n"
    "s_add_u32 m0, s43, 6784\n"
    "v_and_or_b32 v66, v66, s40, v224\n"
    "v_and_or_b32 v67, v67, s40, v224\n"
    "v_mfma_f32_16x16x32_bf16 v[160:163], v[56:59], v[184:187], v[160:163]\n"
    "ds_read_b128 v[180:183], v247 offset:3072\n"
    "v_pk_lshrrev_b16 v68, s41, v71 op_sel:[0,0] op_sel_hi:[1,0]\n"
    "v_pk_lshrrev_b16 v69, s42, v71 op_sel:[0,0] op_sel_hi:[1,0]\n"
    "v_mfma_f32_16x16x32_bf16 v[164:167], v[60:63], v[184:187], v[164:167]\n"
    "v_pk_lshrrev_b16 v70, s41, v71 op_sel:[0,1] op_sel_hi:[1,1]\n"
    "v_pk_lshrrev_b16 v71, s42, v71 op_sel:[0,1] op_sel_hi:[1,1]\n"
    "v_mfma_f32_16x16x32_bf16 v[172:175], v[60:63], v[188:191], v[172:175]\n"
    "ds_read_b128 v[184:187], v244 offset:40960\n"
    "v_and_or_b32 v68, v68, s40, v224\n"
    "v_and_or_b32 v69, v69, s40, v224\n"
    "v_mfma_f32_16x16x32_bf16 v[168:171], v[56:59], v[188:191], v[168:171]\n"
    "s_waitcnt lgkmcnt(3)\n"
    "buffer_load_dwordx4 v250, s[16:19], 0 offen offset:384 lds\n"
    "v_and_or_b32 v70, v70, s40, v224\n"
    "v_and_or_b32 v71, v71, s40, v224\n"
    "v_mfma_f32_16x16x32_bf16 v[144:147], v[64:67], v[192:195], v[144:147]\n"
    "ds_read_b128 v[188:191], v244 offset:59392\n"
    "s_setprio 1\n"
    "s_waitcnt vmcnt(10)\n"
    "v_pk_lshrrev_b16 v72, s41, v78 op_sel:[0,0] op_sel_hi:[1,0]\n"
    "v_pk_lshrrev_b16 v73, s42, v78 op_sel:[0,0] op_sel_hi:[1,0]\n"
    "v_mfma_f32_16x16x32_bf16 v[148:151], v[68:71], v[192:195], v[148:151]\n"
    "v_pk_lshrrev_b16 v74, s41, v78 op_sel:[0,1] op_sel_hi:[1,1]\n"
    "v_pk_lshrrev_b16 v75, s42, v78 op_sel:[0,1] op_sel_hi:[1,1]\n"
    "v_mfma_f32_16x16x32_bf16 v[156:159], v[68:71], v[196:199], v[156:159]\n"
    "ds_read_b128 v[192:195], v245 offset:4096\n"
    "v_and_or_b32 v72, v72, s40, v224\n"
    "v_and_or_b32 v73, v73, s40, v224\n"
    "v_mfma_f32_16x16x32_bf16 v[152:155], v[64:67], v[196:199], v[152:155]\n"
    "s_waitcnt lgkmcnt(3)\n"
    "ds_read_b128 v[196:199], v245 offset:22528\n"
    "buffer_load_dwordx2 v[118:119], v251, s[20:23], s69 offen\n"
    "v_and_or_b32 v74, v74, s40, v224\n"
    "v_and_or_b32 v75, v75, s40, v224\n"
    "v_smfmac_f32_16x16x64_bf16 v[228:231], v[224:227], v[176:183], v241\n"
    "s_waitcnt lgkmcnt(2)\n"
    "ds_read_b128 v[176:179], v245 offset:40960\n"
    "s_setprio 0\n"
    "v_pk_lshrrev_b16 v76, s41, v79 op_sel:[0,0] op_sel_hi:[1,0]\n"
    "v_pk_lshrrev_b16 v77, s42, v79 op_sel:[0,0] op_sel_hi:[1,0]\n"
    "v_mfma_f32_16x16x32_bf16 v[160:163], v[64:67], v[184:187], v[160:163]\n"
    "ds_read_b128 v[180:183], v245 offset:59392\n"
    "v_pk_lshrrev_b16 v78, s41, v79 op_sel:[0,1] op_sel_hi:[1,1]\n"
    "v_pk_lshrrev_b16 v79, s42, v79 op_sel:[0,1] op_sel_hi:[1,1]\n"
    "v_mfma_f32_16x16x32_bf16 v[164:167], v[68:71], v[184:187], v[164:167]\n"
    "buffer_load_dwordx2 v[126:127], v251, s[20:23], s70 offen\n"
    "s_add_u32 m0, s43, 7680\n"
    "v_and_or_b32 v76, v76, s40, v224\n"
    "v_and_or_b32 v77, v77, s40, v224\n"
    "v_mfma_f32_16x16x32_bf16 v[172:175], v[68:71], v[188:191], v[172:175]\n"
    "s_waitcnt vmcnt(8)\n"
    "s_barrier\n"
    "ds_read_b128 v[184:187], v244 offset:5120\n"
    "s_setprio 3\n"
    "v_and_or_b32 v78, v78, s40, v224\n"
    "v_and_or_b32 v79, v79, s40, v224\n"
    "v_mfma_f32_16x16x32_bf16 v[168:171], v[64:67], v[188:191], v[168:171]\n"
    "s_waitcnt lgkmcnt(3)\n"
    "v_mfma_f32_16x16x32_bf16 v[144:147], v[72:75], v[192:195], v[144:147]\n"
    "ds_read_b128 v[188:191], v244 offset:23552\n"
    "s_waitcnt vmcnt(7)\n"
    "v_pk_lshrrev_b16 v80, s41, v86 op_sel:[0,0] op_sel_hi:[1,0]\n"
    "v_pk_lshrrev_b16 v81, s42, v86 op_sel:[0,0] op_sel_hi:[1,0]\n"
    "v_mfma_f32_16x16x32_bf16 v[148:151], v[76:79], v[192:195], v[148:151]\n"
    "buffer_load_dwordx4 v250, s[16:19], 0 offen offset:512 lds\n"
    "v_pk_lshrrev_b16 v82, s41, v86 op_sel:[0,1] op_sel_hi:[1,1]\n"
    "v_pk_lshrrev_b16 v83, s42, v86 op_sel:[0,1] op_sel_hi:[1,1]\n"
    "v_mfma_f32_16x16x32_bf16 v[156:159], v[76:79], v[196:199], v[156:159]\n"
    "ds_read_b128 v[192:195], v244 offset:41984\n"
    "s_setprio 2\n"
    "v_and_or_b32 v80, v80, s40, v224\n"
    "v_and_or_b32 v81, v81, s40, v224\n"
    "v_mfma_f32_16x16x32_bf16 v[152:155], v[72:75], v[196:199], v[152:155]\n"
    "s_waitcnt lgkmcnt(3)\n"
    "v_and_or_b32 v82, v82, s40, v224\n"
    "v_and_or_b32 v83, v83, s40, v224\n"
    "v_mfma_f32_16x16x32_bf16 v[160:163], v[72:75], v[176:179], v[160:163]\n"
    "ds_read_b128 v[196:199], v244 offset:60416\n"
    "v_pk_lshrrev_b16 v84, s41, v87 op_sel:[0,0] op_sel_hi:[1,0]\n"
    "v_pk_lshrrev_b16 v85, s42, v87 op_sel:[0,0] op_sel_hi:[1,0]\n"
    "v_mfma_f32_16x16x32_bf16 v[164:167], v[76:79], v[176:179], v[164:167]\n"
    "buffer_load_dwordx2 v[134:135], v251, s[20:23], s71 offen\n"
    "v_pk_lshrrev_b16 v86, s41, v87 op_sel:[0,1] op_sel_hi:[1,1]\n"
    "v_pk_lshrrev_b16 v87, s42, v87 op_sel:[0,1] op_sel_hi:[1,1]\n"
    "v_mfma_f32_16x16x32_bf16 v[172:175], v[76:79], v[180:183], v[172:175]\n"
    "ds_read_b128 v[176:179], v245 offset:5120\n"
    "s_setprio 1\n"
    "v_and_or_b32 v84, v84, s40, v224\n"
    "v_and_or_b32 v85, v85, s40, v224\n"
    "v_mfma_f32_16x16x32_bf16 v[168:171], v[72:75], v[180:183], v[168:171]\n"
    "s_waitcnt lgkmcnt(3)\n"
    "v_and_or_b32 v86, v86, s40, v224\n"
    "v_and_or_b32 v87, v87, s40, v224\n"
    "v_mfma_f32_16x16x32_bf16 v[144:147], v[80:83], v[184:187], v[144:147]\n"
    "ds_read_b128 v[180:183], v245 offset:23552\n"
    "s_waitcnt vmcnt(8)\n"
    "v_pk_lshrrev_b16 v88, s41, v94 op_sel:[0,0] op_sel_hi:[1,0]\n"
    "v_pk_lshrrev_b16 v89, s42, v94 op_sel:[0,0] op_sel_hi:[1,0]\n"
    "v_mfma_f32_16x16x32_bf16 v[148:151], v[84:87], v[184:187], v[148:151]\n"
    "buffer_load_dwordx2 v[142:143], v251, s[20:23], s72 offen\n"
    "v_add_u32 v250, s74, v250\n"
    "s_add_u32 m0, s43, 73728\n"
    "v_pk_lshrrev_b16 v90, s41, v94 op_sel:[0,1] op_sel_hi:[1,1]\n"
    "v_mfma_f32_16x16x32_bf16 v[156:159], v[84:87], v[188:191], v[156:159]\n"
    "ds_read_b128 v[184:187], v245 offset:41984\n"
    "s_setprio 0\n"
    "v_pk_lshrrev_b16 v91, s42, v94 op_sel:[0,1] op_sel_hi:[1,1]\n"
    "v_and_or_b32 v88, v88, s40, v224\n"
    "v_mfma_f32_16x16x32_bf16 v[152:155], v[80:83], v[188:191], v[152:155]\n"
    "s_waitcnt lgkmcnt(3)\n"
    "v_and_or_b32 v89, v89, s40, v224\n"
    "v_and_or_b32 v90, v90, s40, v224\n"
    "v_mfma_f32_16x16x32_bf16 v[160:163], v[80:83], v[192:195], v[160:163]\n"
    "ds_read_b128 v[188:191], v245 offset:60416\n"
    "v_and_or_b32 v91, v91, s40, v224\n"
    "v_pk_lshrrev_b16 v92, s41, v95 op_sel:[0,0] op_sel_hi:[1,0]\n"
    "v_mfma_f32_16x16x32_bf16 v[164:167], v[84:87], v[192:195], v[164:167]\n"
    "buffer_load_dwordx4 v250, s[16:19], 0 offen offset:0 lds\n"
    "s_add_u32 m0, s43, 74624\n"
    "v_pk_lshrrev_b16 v93, s42, v95 op_sel:[0,0] op_sel_hi:[1,0]\n"
    "v_pk_lshrrev_b16 v94, s41, v95 op_sel:[0,1] op_sel_hi:[1,1]\n"
    "v_mfma_f32_16x16x32_bf16 v[172:175], v[84:87], v[196:199], v[172:175]\n"
    "s_waitcnt vmcnt(9)\n"
    "s_barrier\n"
    "ds_read_b128 v[192:195], v244 offset:6144\n"
    "s_setprio 3\n"
    "v_pk_lshrrev_b16 v95, s42, v95 op_sel:[0,1] op_sel_hi:[1,1]\n"
    "v_and_or_b32 v92, v92, s40, v224\n"
    "v_mfma_f32_16x16x32_bf16 v[168:171], v[80:83], v[196:199], v[168:171]\n"
    "s_waitcnt lgkmcnt(3)\n"
    "v_and_or_b32 v93, v93, s40, v224\n"
    "v_and_or_b32 v94, v94, s40, v224\n"
    "v_and_or_b32 v95, v95, s40, v224\n"
    "v_mfma_f32_16x16x32_bf16 v[144:147], v[88:91], v[176:179], v[144:147]\n"
    "ds_read_b128 v[196:199], v244 offset:24576\n"
    "s_waitcnt vmcnt(8)\n"
    "v_pk_lshrrev_b16 v96, s41, v102 op_sel:[0,0] op_sel_hi:[1,0]\n"
    "v_pk_lshrrev_b16 v97, s42, v102 op_sel:[0,0] op_sel_hi:[1,0]\n"
    "v_mfma_f32_16x16x32_bf16 v[148:151], v[92:95], v[176:179], v[148:151]\n"
    "buffer_load_dwordx4 v250, s[16:19], 0 offen offset:128 lds\n"
    "s_add_u32 m0, s43, 75520\n"
    "v_pk_lshrrev_b16 v98, s41, v102 op_sel:[0,1] op_sel_hi:[1,1]\n"
    "v_pk_lshrrev_b16 v99, s42, v102 op_sel:[0,1] op_sel_hi:[1,1]\n"
    "v_mfma_f32_16x16x32_bf16 v[156:159], v[92:95], v[180:183], v[156:159]\n"
    "ds_read_b128 v[176:179], v247 offset:4096\n"
    "v_and_or_b32 v96, v96, s40, v224\n"
    "v_and_or_b32 v97, v97, s40, v224\n"
    "v_mfma_f32_16x16x32_bf16 v[152:155], v[88:91], v[180:183], v[152:155]\n"
    "s_waitcnt lgkmcnt(3)\n"
    "v_and_or_b32 v98, v98, s40, v224\n"
    "v_and_or_b32 v99, v99, s40, v224\n"
    "v_mfma_f32_16x16x32_bf16 v[160:163], v[88:91], v[184:187], v[160:163]\n"
    "ds_read_b128 v[180:183], v247 offset:5120\n"
    "s_setprio 2\n"
    "v_pk_lshrrev_b16 v100, s41, v103 op_sel:[0,0] op_sel_hi:[1,0]\n"
    "v_pk_lshrrev_b16 v101, s42, v103 op_sel:[0,0] op_sel_hi:[1,0]\n"
    "v_mfma_f32_16x16x32_bf16 v[164:167], v[92:95], v[184:187], v[164:167]\n"
    "buffer_load_dwordx4 v250, s[16:19], 0 offen offset:256 lds\n"
    "s_add_u32 m0, s43, 76416\n"
    "v_pk_lshrrev_b16 v102, s41, v103 op_sel:[0,1] op_sel_hi:[1,1]\n"
    "v_pk_lshrrev_b16 v103, s42, v103 op_sel:[0,1] op_sel_hi:[1,1]\n"
    "v_mfma_f32_16x16x32_bf16 v[172:175], v[92:95], v[188:191], v[172:175]\n"
    "ds_read_b128 v[184:187], v244 offset:43008\n"
    "v_and_or_b32 v100, v100, s40, v224\n"
    "v_and_or_b32 v101, v101, s40, v224\n"
    "v_mfma_f32_16x16x32_bf16 v[168:171], v[88:91], v[188:191], v[168:171]\n"
    "s_waitcnt lgkmcnt(3)\n"
    "v_and_or_b32 v102, v102, s40, v224\n"
    "v_and_or_b32 v103, v103, s40, v224\n"
    "v_mfma_f32_16x16x32_bf16 v[144:147], v[96:99], v[192:195], v[144:147]\n"
    "ds_read_b128 v[188:191], v244 offset:61440\n"
    "s_waitcnt vmcnt(9)\n"
    "v_pk_lshrrev_b16 v104, s41, v110 op_sel:[0,0] op_sel_hi:[1,0]\n"
    "v_pk_lshrrev_b16 v105, s42, v110 op_sel:[0,0] op_sel_hi:[1,0]\n"
    "v_mfma_f32_16x16x32_bf16 v[148:151], v[100:103], v[192:195], v[148:151]\n"
    "buffer_load_dwordx4 v250, s[16:19], 0 offen offset:384 lds\n"
    "v_add_u32 v250, s73, v250\n"
    "s_add_u32 m0, s43, 77824\n"
    "v_pk_lshrrev_b16 v106, s41, v110 op_sel:[0,1] op_sel_hi:[1,1]\n"
    "v_mfma_f32_16x16x32_bf16 v[156:159], v[100:103], v[196:199], v[156:159]\n"
    "ds_read_b128 v[192:195], v245 offset:6144\n"
    "s_setprio 1\n"
    "v_pk_lshrrev_b16 v107, s42, v110 op_sel:[0,1] op_sel_hi:[1,1]\n"
    "v_and_or_b32 v104, v104, s40, v224\n"
    "v_mfma_f32_16x16x32_bf16 v[152:155], v[96:99], v[196:199], v[152:155]\n"
    "s_waitcnt lgkmcnt(3)\n"
    "ds_read_b128 v[196:199], v245 offset:24576\n"
    "v_and_or_b32 v105, v105, s40, v224\n"
    "v_and_or_b32 v106, v106, s40, v224\n"
    "v_smfmac_f32_16x16x64_bf16 v[228:231], v[224:227], v[176:183], v241\n"
    "s_waitcnt lgkmcnt(2)\n"
    "ds_read_b128 v[176:179], v247 offset:6144\n"
    "v_and_or_b32 v107, v107, s40, v224\n"
    "v_pk_lshrrev_b16 v108, s41, v111 op_sel:[0,0] op_sel_hi:[1,0]\n"
    "v_mfma_f32_16x16x32_bf16 v[160:163], v[96:99], v[184:187], v[160:163]\n"
    "s_waitcnt vmcnt(9)\n"
    "ds_read_b128 v[180:183], v247 offset:7168\n"
    "s_setprio 0\n"
    "v_pk_lshrrev_b16 v109, s42, v111 op_sel:[0,0] op_sel_hi:[1,0]\n"
    "v_pk_lshrrev_b16 v110, s41, v111 op_sel:[0,1] op_sel_hi:[1,1]\n"
    "v_mfma_f32_16x16x32_bf16 v[164:167], v[100:103], v[184:187], v[164:167]\n"
    "buffer_load_dwordx4 v250, s[16:19], 0 offen offset:0 lds\n"
    "s_add_u32 m0, s43, 78720\n"
    "v_pk_lshrrev_b16 v111, s42, v111 op_sel:[0,1] op_sel_hi:[1,1]\n"
    "v_and_or_b32 v108, v108, s40, v224\n"
    "v_mfma_f32_16x16x32_bf16 v[172:175], v[100:103], v[188:191], v[172:175]\n"
    "ds_read_b128 v[184:187], v245 offset:43008\n"
    "v_and_or_b32 v109, v109, s40, v224\n"
    "v_and_or_b32 v110, v110, s40, v224\n"
    "v_mfma_f32_16x16x32_bf16 v[168:171], v[96:99], v[188:191], v[168:171]\n"
    "s_waitcnt lgkmcnt(3)\n"
    "v_and_or_b32 v111, v111, s40, v224\n"
    "v_mfma_f32_16x16x32_bf16 v[144:147], v[104:107], v[192:195], v[144:147]\n"
    "ds_read_b128 v[188:191], v245 offset:61440\n"
    "s_waitcnt vmcnt(9)\n"
    "v_pk_lshrrev_b16 v112, s41, v118 op_sel:[0,0] op_sel_hi:[1,0]\n"
    "v_pk_lshrrev_b16 v113, s42, v118 op_sel:[0,0] op_sel_hi:[1,0]\n"
    "v_mfma_f32_16x16x32_bf16 v[148:151], v[108:111], v[192:195], v[148:151]\n"
    "buffer_load_dwordx4 v250, s[16:19], 0 offen offset:128 lds\n"
    "s_add_u32 m0, s43, 79616\n"
    "v_pk_lshrrev_b16 v114, s41, v118 op_sel:[0,1] op_sel_hi:[1,1]\n"
    "v_pk_lshrrev_b16 v115, s42, v118 op_sel:[0,1] op_sel_hi:[1,1]\n"
    "v_mfma_f32_16x16x32_bf16 v[156:159], v[108:111], v[196:199], v[156:159]\n"
    "s_waitcnt vmcnt(8)\n"
    "s_barrier\n"
    "ds_read_b128 v[192:195], v244 offset:7168\n"
    "s_setprio 3\n"
    "v_and_or_b32 v112, v112, s40, v224\n"
    "v_and_or_b32 v113, v113, s40, v224\n"
    "v_mfma_f32_16x16x32_bf16 v[152:155], v[104:107], v[196:199], v[152:155]\n"
    "s_waitcnt lgkmcnt(3)\n"
    "ds_read_b128 v[196:199], v244 offset:25600\n"
    "v_and_or_b32 v114, v114, s40, v224\n"
    "v_and_or_b32 v115, v115, s40, v224\n"
    "v_smfmac_f32_16x16x64_bf16 v[228:231], v[224:227], v[176:183], v241\n"
    "s_waitcnt lgkmcnt(2)\n"
    "ds_read_b128 v[176:179], v247 offset:8192\n"
    "v_pk_lshrrev_b16 v116, s41, v119 op_sel:[0,0] op_sel_hi:[1,0]\n"
    "v_pk_lshrrev_b16 v117, s42, v119 op_sel:[0,0] op_sel_hi:[1,0]\n"
    "v_mfma_f32_16x16x32_bf16 v[160:163], v[104:107], v[184:187], v[160:163]\n"
    "ds_read_b128 v[180:183], v244 offset:44032\n"
    "v_pk_lshrrev_b16 v118, s41, v119 op_sel:[0,1] op_sel_hi:[1,1]\n"
    "v_pk_lshrrev_b16 v119, s42, v119 op_sel:[0,1] op_sel_hi:[1,1]\n"
    "v_mfma_f32_16x16x32_bf16 v[164:167], v[108:111], v[184:187], v[164:167]\n"
    "buffer_load_dwordx4 v250, s[16:19], 0 offen offset:256 lds\n"
    "s_add_u32 m0, s43, 80512\n"
    "v_and_or_b32 v116, v116, s40, v224\n"
    "v_and_or_b32 v117, v117, s40, v224\n"
    "v_mfma_f32_16x16x32_bf16 v[172:175], v[108:111], v[188:191], v[172:175]\n"
    "ds_read_b128 v[184:187], v244 offset:62464\n"
    "v_and_or_b32 v118, v118, s40, v224\n"
    "v_and_or_b32 v119, v119, s40, v224\n"
    "v_mfma_f32_16x16x32_bf16 v[168:171], v[104:107], v[188:191], v[168:171]\n"
    "s_waitcnt lgkmcnt(3)\n"
    "buffer_load_dwordx4 v250, s[16:19], 0 offen offset:384 lds\n"
    "s_add_u32 m0, s43, 81408\n"
    "v_mfma_f32_16x16x32_bf16 v[144:147], v[112:115], v[192:195], v[144:147]\n"
    "ds_read_b128 v[188:191], v245 offset:7168\n"
    "v_pk_lshrrev_b16 v120, s41, v126 op_sel:[0,0] op_sel_hi:[1,0]\n"
    "v_pk_lshrrev_b16 v121, s42, v126 op_sel:[0,0] op_sel_hi:[1,0]\n"
    "v_mfma_f32_16x16x32_bf16 v[148:151], v[116:119], v[192:195], v[148:151]\n"
    "v_pk_lshrrev_b16 v122, s41, v126 op_sel:[0,1] op_sel_hi:[1,1]\n"
    "v_pk_lshrrev_b16 v123, s42, v126 op_sel:[0,1] op_sel_hi:[1,1]\n"
    "v_mfma_f32_16x16x32_bf16 v[156:159], v[116:119], v[196:199], v[156:159]\n"
    "ds_read_b128 v[192:195], v245 offset:25600\n"
    "v_and_or_b32 v120, v120, s40, v224\n"
    "v_and_or_b32 v121, v121, s40, v224\n"
    "v_mfma_f32_16x16x32_bf16 v[152:155], v[112:115], v[196:199], v[152:155]\n"
    "s_waitcnt lgkmcnt(3)\n"
    "buffer_load_dwordx4 v250, s[16:19], 0 offen offset:512 lds\n"
    "v_and_or_b32 v122, v122, s40, v224\n"
    "v_and_or_b32 v123, v123, s40, v224\n"
    "v_smfmac_f32_16x16x32_bf16 v[228:231], v[224:225], v[176:179], v241\n"
    "ds_read_b128 v[196:199], v245 offset:44032\n"
    "s_setprio 2\n"
    "v_pk_lshrrev_b16 v124, s41, v127 op_sel:[0,0] op_sel_hi:[1,0]\n"
    "v_pk_lshrrev_b16 v125, s42, v127 op_sel:[0,0] op_sel_hi:[1,0]\n"
    "v_mfma_f32_16x16x32_bf16 v[160:163], v[112:115], v[180:183], v[160:163]\n"
    "ds_read_b128 v[176:179], v245 offset:62464\n"
    "v_pk_lshrrev_b16 v126, s41, v127 op_sel:[0,1] op_sel_hi:[1,1]\n"
    "v_pk_lshrrev_b16 v127, s42, v127 op_sel:[0,1] op_sel_hi:[1,1]\n"
    "v_mfma_f32_16x16x32_bf16 v[164:167], v[116:119], v[180:183], v[164:167]\n"
    "s_waitcnt lgkmcnt(3)\n"
    "v_and_or_b32 v124, v124, s40, v224\n"
    "v_and_or_b32 v125, v125, s40, v224\n"
    "v_mfma_f32_16x16x32_bf16 v[172:175], v[116:119], v[184:187], v[172:175]\n"
    "ds_read_b128 v[180:183], v244 offset:8192\n"
    "v_and_or_b32 v126, v126, s40, v224\n"
    "v_and_or_b32 v127, v127, s40, v224\n"
    "v_mfma_f32_16x16x32_bf16 v[168:171], v[112:115], v[184:187], v[168:171]\n"
    "v_lshlrev_b32 v232, 16, v233\n"
    "v_and_b32 v233, s49, v233\n"
    "v_fma_f32 v232, v232, v236, s57\n"
    "v_fma_f32 v233, v233, v236, s57\n"
    "v_mfma_f32_16x16x32_bf16 v[144:147], v[120:123], v[188:191], v[144:147]\n"
    "ds_read_b128 v[184:187], v244 offset:26624\n"
    "v_add_f32 v230, v228, v229\n"
    "v_mov_b32 v228, 0\n"
    "v_mfma_f32_16x16x32_bf16 v[148:151], v[124:127], v[188:191], v[148:151]\n"
    "s_waitcnt lgkmcnt(3)\n"
    "v_mov_b32 v229, 0\n"
    "s_waitcnt vmcnt(10)\n"
    "v_pk_lshrrev_b16 v128, s41, v134 op_sel:[0,0] op_sel_hi:[1,0]\n"
    "v_mfma_f32_16x16x32_bf16 v[156:159], v[124:127], v[192:195], v[156:159]\n"
    "ds_read_b128 v[188:191], v244 offset:45056\n"
    "v_pk_lshrrev_b16 v129, s42, v134 op_sel:[0,0] op_sel_hi:[1,0]\n"
    "v_pk_lshrrev_b16 v130, s41, v134 op_sel:[0,1] op_sel_hi:[1,1]\n"
    "v_mfma_f32_16x16x32_bf16 v[152:155], v[120:123], v[192:195], v[152:155]\n"
    "v_pk_lshrrev_b16 v131, s42, v134 op_sel:[0,1] op_sel_hi:[1,1]\n"
    "v_add_f32_dpp v231, v230, v230 row_ror:8 row_mask:0xf bank_mask:0xf\n"
    "v_mfma_f32_16x16x32_bf16 v[160:163], v[120:123], v[196:199], v[160:163]\n"
    "ds_read_b128 v[192:195], v244 offset:63488\n"
    "s_setprio 1\n"
    "v_add_u32 v247, s64, v247\n"
    "v_and_or_b32 v128, v128, s40, v224\n"
    "v_mfma_f32_16x16x32_bf16 v[164:167], v[124:127], v[196:199], v[164:167]\n"
    "s_waitcnt lgkmcnt(3)\n"
    "v_and_or_b32 v129, v129, s40, v224\n"
    "v_and_or_b32 v130, v130, s40, v224\n"
    "v_mfma_f32_16x16x32_bf16 v[172:175], v[124:127], v[176:179], v[172:175]\n"
    "ds_read_b128 v[196:199], v245 offset:8192\n"
    "v_and_or_b32 v131, v131, s40, v224\n"
    "v_pk_lshrrev_b16 v132, s41, v135 op_sel:[0,0] op_sel_hi:[1,0]\n"
    "v_mfma_f32_16x16x32_bf16 v[168:171], v[120:123], v[176:179], v[168:171]\n"
    "v_pk_lshrrev_b16 v133, s42, v135 op_sel:[0,0] op_sel_hi:[1,0]\n"
    "v_pk_lshrrev_b16 v134, s41, v135 op_sel:[0,1] op_sel_hi:[1,1]\n"
    "v_pk_lshrrev_b16 v135, s42, v135 op_sel:[0,1] op_sel_hi:[1,1]\n"
    "v_and_or_b32 v132, v132, s40, v224\n"
    "v_and_or_b32 v133, v133, s40, v224\n"
    "v_and_or_b32 v134, v134, s40, v224\n"
    "v_and_or_b32 v135, v135, s40, v224\n"
    "v_mfma_f32_16x16x32_bf16 v[144:147], v[128:131], v[180:183], v[144:147]\n"
    "ds_read_b128 v[176:179], v245 offset:26624\n"
    "s_waitcnt vmcnt(9)\n"
    "v_pk_lshrrev_b16 v136, s41, v142 op_sel:[0,0] op_sel_hi:[1,0]\n"
    "v_pk_lshrrev_b16 v137, s42, v142 op_sel:[0,0] op_sel_hi:[1,0]\n"
    "v_mfma_f32_16x16x32_bf16 v[148:151], v[132:135], v[180:183], v[148:151]\n"
    "s_waitcnt lgkmcnt(3)\n"
    "v_pk_lshrrev_b16 v138, s41, v142 op_sel:[0,1] op_sel_hi:[1,1]\n"
    "v_pk_lshrrev_b16 v139, s42, v142 op_sel:[0,1] op_sel_hi:[1,1]\n"
    "v_mfma_f32_16x16x32_bf16 v[156:159], v[132:135], v[184:187], v[156:159]\n"
    "ds_read_b128 v[180:183], v245 offset:45056\n"
    "s_setprio 0\n"
    "v_and_or_b32 v136, v136, s40, v224\n"
    "v_and_or_b32 v137, v137, s40, v224\n"
    "v_mfma_f32_16x16x32_bf16 v[152:155], v[128:131], v[184:187], v[152:155]\n"
    "s_waitcnt vmcnt(0)\n"
    "ds_write2_b32 v235, v232, v233 offset0:0 offset1:4\n"
    "ds_write_b32 v254, v231 offset:0\n"
    "v_and_or_b32 v138, v138, s40, v224\n"
    "v_and_or_b32 v139, v139, s40, v224\n"
    "v_mfma_f32_16x16x32_bf16 v[160:163], v[128:131], v[188:191], v[160:163]\n"
    "ds_read_b128 v[184:187], v245 offset:63488\n"
    "v_pk_lshrrev_b16 v140, s41, v143 op_sel:[0,0] op_sel_hi:[1,0]\n"
    "v_pk_lshrrev_b16 v141, s42, v143 op_sel:[0,0] op_sel_hi:[1,0]\n"
    "v_mfma_f32_16x16x32_bf16 v[164:167], v[132:135], v[188:191], v[164:167]\n"
    "s_waitcnt lgkmcnt(5)\n"
    "v_pk_lshrrev_b16 v142, s41, v143 op_sel:[0,1] op_sel_hi:[1,1]\n"
    "v_pk_lshrrev_b16 v143, s42, v143 op_sel:[0,1] op_sel_hi:[1,1]\n"
    "v_mfma_f32_16x16x32_bf16 v[172:175], v[132:135], v[192:195], v[172:175]\n"
    "v_add_u32 v244, s64, v244\n"
    "v_add_u32 v245, s64, v245\n"
    "s_waitcnt lgkmcnt(2)\n"
    "s_waitcnt lgkmcnt(1)\n"
    "s_barrier\n"
    "ds_read_b128 v[188:191], v244 offset:0\n"
    "s_setprio 3\n"
    "v_and_or_b32 v140, v140, s40, v224\n"
    "v_and_or_b32 v141, v141, s40, v224\n"
    "v_mfma_f32_16x16x32_bf16 v[168:171], v[128:131], v[192:195], v[168:171]\n"
    "v_and_or_b32 v142, v142, s40, v224\n"
    "v_and_or_b32 v143, v143, s40, v224\n"
    "v_mfma_f32_16x16x32_bf16 v[144:147], v[136:139], v[196:199], v[144:147]\n"
    "ds_read_b128 v[192:195], v245 offset:0\n"
    "s_lshl_b32 s44, %2, 7\n"
    "v_add_u32 v255, s44, v253\n"
    "v_mfma_f32_16x16x32_bf16 v[148:151], v[140:143], v[196:199], v[148:151]\n"
    "v_mfma_f32_16x16x32_bf16 v[156:159], v[140:143], v[176:179], v[156:159]\n"
    "ds_read_b128 v[196:199], v244 offset:1024\n"
    "ds_read_b128 v[208:211], v255 offset:1408\n"
    "ds_read_b128 v[212:215], v255 offset:1424\n"
    "v_mfma_f32_16x16x32_bf16 v[152:155], v[136:139], v[176:179], v[152:155]\n"
    "v_mfma_f32_16x16x32_bf16 v[160:163], v[136:139], v[180:183], v[160:163]\n"
    "ds_read_b128 v[176:179], v245 offset:1024\n"
    "ds_read_b32 v242, v249 offset:0\n"
    "ds_read_b128 v[200:203], v255 offset:0\n"
    "v_mfma_f32_16x16x32_bf16 v[164:167], v[140:143], v[180:183], v[164:167]\n"
    "s_waitcnt lgkmcnt(7)\n"
    "v_mfma_f32_16x16x32_bf16 v[172:175], v[140:143], v[184:187], v[172:175]\n"
    "ds_read_b128 v[180:183], v244 offset:2048\n"
    "ds_read_b128 v[204:207], v255 offset:16\n"
    "ds_read_b32 v243, v249 offset:64\n"
    "v_mfma_f32_16x16x32_bf16 v[168:171], v[136:139], v[184:187], v[168:171]\n"
    "v_mfma_f32_16x16x32_bf16 v[216:219], v[0:3], v[188:191], 0\n"
    "ds_read_b128 v[184:187], v247 offset:0\n"
    "ds_read2st64_b32 v[250:251], v246 offset0:0 offset1:4\n"
    "v_mfma_f32_16x16x32_bf16 v[220:223], v[4:7], v[188:191], 0\n"
    "s_waitcnt lgkmcnt(10)\n"
    "v_mfma_f32_16x16x32_bf16 v[216:219], v[8:11], v[192:195], v[216:219]\n"
    "ds_read_b128 v[188:191], v247 offset:1024\n"
    "s_waitcnt lgkmcnt(7)\n"
    "v_fmac_f32 v144, v208, v242\n"
    "v_fmac_f32 v145, v209, v242\n"
    "v_mfma_f32_16x16x32_bf16 v[220:223], v[12:15], v[192:195], v[220:223]\n"
    "v_fmac_f32 v146, v210, v242\n"
    "v_fmac_f32 v147, v211, v242\n"
    "v_mfma_f32_16x16x32_bf16 v[216:219], v[16:19], v[196:199], v[216:219]\n"
    "ds_read_b128 v[192:195], v245 offset:2048\n"
    "v_fmac_f32 v148, v212, v242\n"
    "v_fmac_f32 v149, v213, v242\n"
    "v_mfma_f32_16x16x32_bf16 v[220:223], v[20:23], v[196:199], v[220:223]\n"
    "s_waitcnt lgkmcnt(6)\n"
    "v_fmac_f32 v150, v214, v242\n"
    "v_fmac_f32 v151, v215, v242\n"
    "v_mfma_f32_16x16x32_bf16 v[216:219], v[24:27], v[176:179], v[216:219]\n"
    "ds_read_b128 v[196:199], v244 offset:3072\n"
    "v_mul_f32 v144, v200, v144\n"
    "v_mul_f32 v145, v201, v145\n"
    "v_mfma_f32_16x16x32_bf16 v[220:223], v[28:31], v[176:179], v[220:223]\n"
    "v_mul_f32 v146, v202, v146\n"
    "v_mul_f32 v147, v203, v147\n"
    "v_mfma_f32_16x16x32_bf16 v[216:219], v[32:35], v[180:183], v[216:219]\n"
    "ds_read_b128 v[176:179], v245 offset:3072\n"
    "s_waitcnt lgkmcnt(7)\n"
    "v_mul_f32 v148, v204, v148\n"
    "v_mul_f32 v149, v205, v149\n"
    "v_mfma_f32_16x16x32_bf16 v[220:223], v[36:39], v[180:183], v[220:223]\n"
    "s_waitcnt lgkmcnt(3)\n"
    "ds_read_b128 v[180:183], v244 offset:4096\n"
    "v_mul_f32 v150, v206, v150\n"
    "v_mul_f32 v151, v207, v151\n"
    "v_smfmac_f32_16x16x64_bf16 v[228:231], v[224:227], v[184:191], v241\n"
    "s_waitcnt lgkmcnt(2)\n"
    "ds_read_b128 v[184:187], v247 offset:2048\n"
    "v_cvt_pk_bf16_f32 v144, v144, v148\n"
    "v_cvt_pk_bf16_f32 v145, v145, v149\n"
    "v_mfma_f32_16x16x32_bf16 v[216:219], v[40:43], v[192:195], v[216:219]\n"
    "ds_read_b128 v[188:191], v247 offset:3072\n"
    "v_cvt_pk_bf16_f32 v146, v146, v150\n"
    "v_cvt_pk_bf16_f32 v147, v147, v151\n"
    "v_mfma_f32_16x16x32_bf16 v[220:223], v[44:47], v[192:195], v[220:223]\n"
    "buffer_store_dwordx4 v[144:147], v248, s[36:39], 0 offen sc1\n"
    "ds_read_b32 v242, v249 offset:128\n"
    "v_fmac_f32 v152, v208, v243\n"
    "v_fmac_f32 v153, v209, v243\n"
    "v_mfma_f32_16x16x32_bf16 v[216:219], v[48:51], v[196:199], v[216:219]\n"
    "ds_read_b128 v[192:195], v245 offset:4096\n"
    "v_fmac_f32 v154, v210, v243\n"
    "v_fmac_f32 v155, v211, v243\n"
    "v_mfma_f32_16x16x32_bf16 v[220:223], v[52:55], v[196:199], v[220:223]\n"
    "s_waitcnt lgkmcnt(4)\n"
    "v_fmac_f32 v156, v212, v243\n"
    "v_fmac_f32 v157, v213, v243\n"
    "v_mfma_f32_16x16x32_bf16 v[216:219], v[56:59], v[176:179], v[216:219]\n"
    "ds_read_b128 v[196:199], v244 offset:5120\n"
    "v_fmac_f32 v158, v214, v243\n"
    "v_fmac_f32 v159, v215, v243\n"
    "v_mfma_f32_16x16x32_bf16 v[220:223], v[60:63], v[176:179], v[220:223]\n"
    "v_mul_f32 v152, v200, v152\n"
    "v_mul_f32 v153, v201, v153\n"
    "v_mfma_f32_16x16x32_bf16 v[216:219], v[64:67], v[180:183], v[216:219]\n"
    "ds_read_b128 v[176:179], v240 offset:0\n"
    "v_mul_f32 v154, v202, v154\n"
    "v_mul_f32 v155, v203, v155\n"
    "v_mfma_f32_16x16x32_bf16 v[220:223], v[68:71], v[180:183], v[220:223]\n"
    "s_waitcnt lgkmcnt(4)\n"
    "ds_read_b128 v[180:183], v240 offset:18432\n"
    "v_mul_f32 v156, v204, v156\n"
    "v_mul_f32 v157, v205, v157\n"
    "v_smfmac_f32_16x16x64_bf16 v[228:231], v[224:227], v[184:191], v241\n"
    "s_waitcnt lgkmcnt(2)\n"
    "ds_read_b128 v[184:187], v245 offset:5120\n"
    "v_mul_f32 v158, v206, v158\n"
    "v_mul_f32 v159, v207, v159\n"
    "v_mfma_f32_16x16x32_bf16 v[216:219], v[72:75], v[192:195], v[216:219]\n"
    "ds_read_b128 v[188:191], v240 offset:36864\n"
    "v_cvt_pk_bf16_f32 v152, v152, v156\n"
    "v_cvt_pk_bf16_f32 v153, v153, v157\n"
    "v_mfma_f32_16x16x32_bf16 v[220:223], v[76:79], v[192:195], v[220:223]\n"
    "v_cvt_pk_bf16_f32 v154, v154, v158\n"
    "v_cvt_pk_bf16_f32 v155, v155, v159\n"
    "v_mfma_f32_16x16x32_bf16 v[216:219], v[80:83], v[196:199], v[216:219]\n"
    "ds_read_b128 v[192:195], v240 offset:55296\n"
    "buffer_store_dwordx4 v[152:155], v248, s[36:39], s65 offen sc1\n"
    "ds_read_b32 v243, v249 offset:192\n"
    "v_pk_lshrrev_b16 v232, s41, v250 op_sel:[0,0] op_sel_hi:[1,0]\n"
    "v_pk_lshrrev_b16 v233, s42, v250 op_sel:[0,0] op_sel_hi:[1,0]\n"
    "v_mfma_f32_16x16x32_bf16 v[220:223], v[84:87], v[196:199], v[220:223]\n"
    "s_waitcnt lgkmcnt(4)\n"
    "v_pk_lshrrev_b16 v234, s41, v250 op_sel:[0,1] op_sel_hi:[1,1]\n"
    "v_pk_lshrrev_b16 v235, s42, v250 op_sel:[0,1] op_sel_hi:[1,1]\n"
    "v_and_or_b32 v232, v232, s40, v224\n"
    "v_and_or_b32 v233, v233, s40, v224\n"
    "v_and_or_b32 v234, v234, s40, v224\n"
    "v_and_or_b32 v235, v235, s40, v224\n"
    "s_nop 1\n"
    "v_mfma_f32_16x16x32_bf16 v[144:147], v[232:235], v[176:179], 0\n"
    "ds_read_b128 v[196:199], v244 offset:6144\n"
    "v_pk_lshrrev_b16 v236, s41, v251 op_sel:[0,0] op_sel_hi:[1,0]\n"
    "v_pk_lshrrev_b16 v237, s42, v251 op_sel:[0,0] op_sel_hi:[1,0]\n"
    "v_mfma_f32_16x16x32_bf16 v[148:151], v[232:235], v[180:183], 0\n"
    "s_waitcnt lgkmcnt(3)\n"
    "ds_read_b128 v[176:179], v247 offset:4096\n"
    "v_pk_lshrrev_b16 v238, s41, v251 op_sel:[0,1] op_sel_hi:[1,1]\n"
    "v_pk_lshrrev_b16 v239, s42, v251 op_sel:[0,1] op_sel_hi:[1,1]\n"
    "v_mfma_f32_16x16x32_bf16 v[216:219], v[88:91], v[184:187], v[216:219]\n"
    "ds_read_b128 v[180:183], v247 offset:5120\n"
    "ds_read2st64_b32 v[250:251], v246 offset0:8 offset1:12\n"
    "v_and_or_b32 v236, v236, s40, v224\n"
    "v_and_or_b32 v237, v237, s40, v224\n"
    "v_mfma_f32_16x16x32_bf16 v[220:223], v[92:95], v[184:187], v[220:223]\n"
    "v_and_or_b32 v238, v238, s40, v224\n"
    "v_and_or_b32 v239, v239, s40, v224\n"
    "v_mfma_f32_16x16x32_bf16 v[152:155], v[232:235], v[188:191], 0\n"
    "s_waitcnt lgkmcnt(3)\n"
    "ds_read_b128 v[184:187], v240 offset:1024\n"
    "v_fmac_f32 v160, v208, v242\n"
    "v_fmac_f32 v161, v209, v242\n"
    "v_mfma_f32_16x16x32_bf16 v[156:159], v[232:235], v[192:195], 0\n"
    "ds_read_b128 v[188:191], v240 offset:19456\n"
    "v_fmac_f32 v162, v210, v242\n"
    "v_fmac_f32 v163, v211, v242\n"
    "v_mfma_f32_16x16x32_bf16 v[216:219], v[96:99], v[196:199], v[216:219]\n"
    "ds_read_b128 v[192:195], v245 offset:6144\n"
    "v_fmac_f32 v164, v212, v242\n"
    "v_fmac_f32 v165, v213, v242\n"
    "v_mfma_f32_16x16x32_bf16 v[220:223], v[100:103], v[196:199], v[220:223]\n"
    "s_waitcnt lgkmcnt(4)\n"
    "ds_read_b128 v[196:199], v240 offset:37888\n"
    "v_smfmac_f32_16x16x64_bf16 v[228:231], v[224:227], v[176:183], v241\n"
    "s_waitcnt lgkmcnt(2)\n"
    "ds_read_b128 v[176:179], v240 offset:56320\n"
    "v_pk_lshrrev_b16 v232, s41, v250 op_sel:[0,0] op_sel_hi:[1,0]\n"
    "v_pk_lshrrev_b16 v233, s42, v250 op_sel:[0,0] op_sel_hi:[1,0]\n"
    "v_mfma_f32_16x16x32_bf16 v[144:147], v[236:239], v[184:187], v[144:147]\n"
    "ds_read_b128 v[180:183], v244 offset:7168\n"
    "v_pk_lshrrev_b16 v234, s41, v250 op_sel:[0,1] op_sel_hi:[1,1]\n"
    "v_pk_lshrrev_b16 v235, s42, v250 op_sel:[0,1] op_sel_hi:[1,1]\n"
    "v_mfma_f32_16x16x32_bf16 v[148:151], v[236:239], v[188:191], v[148:151]\n"
    "s_waitcnt lgkmcnt(2)\n"
    "ds_read_b128 v[184:187], v240 offset:2048\n"
    "v_and_or_b32 v232, v232, s40, v224\n"
    "v_and_or_b32 v233, v233, s40, v224\n"
    "v_mfma_f32_16x16x32_bf16 v[216:219], v[104:107], v[192:195], v[216:219]\n"
    "ds_read_b128 v[188:191], v240 offset:20480\n"
    "v_and_or_b32 v234, v234, s40, v224\n"
    "v_and_or_b32 v235, v235, s40, v224\n"
    "v_mfma_f32_16x16x32_bf16 v[220:223], v[108:111], v[192:195], v[220:223]\n"
    "v_fmac_f32 v166, v214, v242\n"
    "v_fmac_f32 v167, v215, v242\n"
    "v_mfma_f32_16x16x32_bf16 v[152:155], v[236:239], v[196:199], v[152:155]\n"
    "s_waitcnt lgkmcnt(2)\n"
    "ds_read_b128 v[192:195], v245 offset:7168\n"
    "v_mul_f32 v160, v200, v160\n"
    "v_mul_f32 v161, v201, v161\n"
    "v_mfma_f32_16x16x32_bf16 v[156:159], v[236:239], v[176:179], v[156:159]\n"
    "ds_read_b128 v[196:199], v240 offset:38912\n"
    "v_mul_f32 v162, v202, v162\n"
    "v_mul_f32 v163, v203, v163\n"
    "v_mfma_f32_16x16x32_bf16 v[216:219], v[112:115], v[180:183], v[216:219]\n"
    "ds_read_b128 v[176:179], v240 offset:57344\n"
    "v_pk_lshrrev_b16 v236, s41, v251 op_sel:[0,0] op_sel_hi:[1,0]\n"
    "v_pk_lshrrev_b16 v237, s42, v251 op_sel:[0,0] op_sel_hi:[1,0]\n"
    "v_mfma_f32_16x16x32_bf16 v[220:223], v[116:119], v[180:183], v[220:223]\n"
    "s_waitcnt lgkmcnt(3)\n"
    "v_pk_lshrrev_b16 v238, s41, v251 op_sel:[0,1] op_sel_hi:[1,1]\n"
    "v_pk_lshrrev_b16 v239, s42, v251 op_sel:[0,1] op_sel_hi:[1,1]\n"
    "v_mfma_f32_16x16x32_bf16 v[144:147], v[232:235], v[184:187], v[144:147]\n"
    "ds_read_b128 v[180:183], v244 offset:8192\n"
    "s_setprio 2\n"
    "ds_read2st64_b32 v[250:251], v246 offset0:16 offset1:20\n"
    "v_and_or_b32 v236, v236, s40, v224\n"
    "v_and_or_b32 v237, v237, s40, v224\n"
    "v_mfma_f32_16x16x32_bf16 v[148:151], v[232:235], v[188:191], v[148:151]\n"
    "s_waitcnt lgkmcnt(3)\n"
    "ds_read_b128 v[184:187], v247 offset:6144\n"
    "v_and_or_b32 v238, v238, s40, v224\n"
    "v_and_or_b32 v239, v239, s40, v224\n"
    "v_mfma_f32_16x16x32_bf16 v[216:219], v[120:123], v[192:195], v[216:219]\n"
    "ds_read_b128 v[188:191], v247 offset:7168\n"
    "v_mul_f32 v164, v204, v164\n"
    "v_mul_f32 v165, v205, v165\n"
    "v_mfma_f32_16x16x32_bf16 v[220:223], v[124:127], v[192:195], v[220:223]\n"
    "v_mul_f32 v166, v206, v166\n"
    "v_mul_f32 v167, v207, v167\n"
    "v_mfma_f32_16x16x32_bf16 v[152:155], v[232:235], v[196:199], v[152:155]\n"
    "s_waitcnt lgkmcnt(3)\n"
    "ds_read_b128 v[192:195], v240 offset:3072\n"
    "v_cvt_pk_bf16_f32 v160, v160, v164\n"
    "v_cvt_pk_bf16_f32 v161, v161, v165\n"
    "v_mfma_f32_16x16x32_bf16 v[156:159], v[232:235], v[176:179], v[156:159]\n"
    "ds_read_b128 v[196:199], v240 offset:21504\n"
    "v_mfma_f32_16x16x32_bf16 v[216:219], v[128:131], v[180:183], v[216:219]\n"
    "ds_read_b128 v[176:179], v245 offset:8192\n"
    "v_mfma_f32_16x16x32_bf16 v[220:223], v[132:135], v[180:183], v[220:223]\n"
    "s_waitcnt lgkmcnt(3)\n"
    "ds_read_b128 v[180:183], v240 offset:39936\n"
    "v_smfmac_f32_16x16x64_bf16 v[228:231], v[224:227], v[184:191], v241\n"
    "s_waitcnt lgkmcnt(2)\n"
    "ds_read_b128 v[184:187], v240 offset:58368\n"
    "v_pk_lshrrev_b16 v232, s41, v250 op_sel:[0,0] op_sel_hi:[1,0]\n"
    "v_pk_lshrrev_b16 v233, s42, v250 op_sel:[0,0] op_sel_hi:[1,0]\n"
    "v_mfma_f32_16x16x32_bf16 v[144:147], v[236:239], v[192:195], v[144:147]\n"
    "ds_read_b128 v[188:191], v244 offset:18432\n"
    "v_pk_lshrrev_b16 v234, s41, v250 op_sel:[0,1] op_sel_hi:[1,1]\n"
    "v_pk_lshrrev_b16 v235, s42, v250 op_sel:[0,1] op_sel_hi:[1,1]\n"
    "v_mfma_f32_16x16x32_bf16 v[148:151], v[236:239], v[196:199], v[148:151]\n"
    "s_waitcnt lgkmcnt(2)\n"
    "ds_read_b128 v[192:195], v240 offset:4096\n"
    "v_and_or_b32 v232, v232, s40, v224\n"
    "v_and_or_b32 v233, v233, s40, v224\n"
    "v_mfma_f32_16x16x32_bf16 v[216:219], v[136:139], v[176:179], v[216:219]\n"
    "ds_read_b128 v[196:199], v240 offset:22528\n"
    "v_and_or_b32 v234, v234, s40, v224\n"
    "v_and_or_b32 v235, v235, s40, v224\n"
    "v_mfma_f32_16x16x32_bf16 v[220:223], v[140:143], v[176:179], v[220:223]\n"
    "v_cvt_pk_bf16_f32 v162, v162, v166\n"
    "v_cvt_pk_bf16_f32 v163, v163, v167\n"
    "v_mfma_f32_16x16x32_bf16 v[152:155], v[236:239], v[180:183], v[152:155]\n"
    "s_waitcnt lgkmcnt(2)\n"
    "ds_read_b128 v[176:179], v245 offset:18432\n"
    "buffer_store_dwordx4 v[160:163], v248, s[36:39], s67 offen sc1\n"
    "v_fmac_f32 v168, v208, v243\n"
    "v_fmac_f32 v169, v209, v243\n"
    "v_mfma_f32_16x16x32_bf16 v[156:159], v[236:239], v[184:187], v[156:159]\n"
    "ds_read_b128 v[180:183], v240 offset:40960\n"
    "v_fmac_f32 v170, v210, v243\n"
    "v_fmac_f32 v171, v211, v243\n"
    "v_mfma_f32_16x16x32_bf16 v[160:163], v[0:3], v[188:191], 0\n"
    "ds_read_b128 v[184:187], v240 offset:59392\n"
    "v_pk_lshrrev_b16 v236, s41, v251 op_sel:[0,0] op_sel_hi:[1,0]\n"
    "v_pk_lshrrev_b16 v237, s42, v251 op_sel:[0,0] op_sel_hi:[1,0]\n"
    "v_mfma_f32_16x16x32_bf16 v[164:167], v[4:7], v[188:191], 0\n"
    "s_waitcnt lgkmcnt(3)\n"
    "v_pk_lshrrev_b16 v238, s41, v251 op_sel:[0,1] op_sel_hi:[1,1]\n"
    "v_pk_lshrrev_b16 v239, s42, v251 op_sel:[0,1] op_sel_hi:[1,1]\n"
    "v_mfma_f32_16x16x32_bf16 v[144:147], v[232:235], v[192:195], v[144:147]\n"
    "ds_read_b128 v[188:191], v244 offset:19456\n"
    "ds_read2st64_b32 v[250:251], v246 offset0:24 offset1:28\n"
    "v_and_or_b32 v236, v236, s40, v224\n"
    "v_and_or_b32 v237, v237, s40, v224\n"
    "v_mfma_f32_16x16x32_bf16 v[148:151], v[232:235], v[196:199], v[148:151]\n"
    "s_waitcnt lgkmcnt(3)\n"
    "ds_read_b128 v[192:195], v247 offset:8192\n"
    "v_and_or_b32 v238, v238, s40, v224\n"
    "v_and_or_b32 v239, v239, s40, v224\n"
    "v_mfma_f32_16x16x32_bf16 v[160:163], v[8:11], v[176:179], v[160:163]\n"
    "ds_read_b128 v[196:199], v240 offset:5120\n"
    "v_fmac_f32 v172, v212, v243\n"
    "v_fmac_f32 v173, v213, v243\n"
    "v_mfma_f32_16x16x32_bf16 v[164:167], v[12:15], v[176:179], v[164:167]\n"
    "v_fmac_f32 v174, v214, v243\n"
    "v_fmac_f32 v175, v215, v243\n"
    "v_mfma_f32_16x16x32_bf16 v[152:155], v[232:235], v[180:183], v[152:155]\n"
    "s_waitcnt lgkmcnt(3)\n"
    "ds_read_b128 v[176:179], v240 offset:23552\n"
    "v_mul_f32 v168, v200, v168\n"
    "v_mul_f32 v169, v201, v169\n"
    "v_mfma_f32_16x16x32_bf16 v[156:159], v[232:235], v[184:187], v[156:159]\n"
    "ds_read_b128 v[180:183], v245 offset:19456\n"
    "v_mfma_f32_16x16x32_bf16 v[160:163], v[16:19], v[188:191], v[160:163]\n"
    "ds_read_b128 v[184:187], v240 offset:41984\n"
    "v_mfma_f32_16x16x32_bf16 v[164:167], v[20:23], v[188:191], v[164:167]\n"
    "s_waitcnt lgkmcnt(3)\n"
    "v_smfmac_f32_16x16x32_bf16 v[228:231], v[224:225], v[192:195], v241\n"
    "ds_read_b128 v[188:191], v240 offset:60416\n"
    "v_pk_lshrrev_b16 v232, s41, v250 op_sel:[0,0] op_sel_hi:[1,0]\n"
    "v_pk_lshrrev_b16 v233, s42, v250 op_sel:[0,0] op_sel_hi:[1,0]\n"
    "v_mfma_f32_16x16x32_bf16 v[144:147], v[236:239], v[196:199], v[144:147]\n"
    "s_waitcnt lgkmcnt(2)\n"
    "ds_read_b128 v[192:195], v244 offset:20480\n"
    "v_pk_lshrrev_b16 v234, s41, v250 op_sel:[0,1] op_sel_hi:[1,1]\n"
    "v_pk_lshrrev_b16 v235, s42, v250 op_sel:[0,1] op_sel_hi:[1,1]\n"
    "v_mfma_f32_16x16x32_bf16 v[148:151], v[236:239], v[176:179], v[148:151]\n"
    "ds_read_b128 v[196:199], v240 offset:6144\n"
    "s_setprio 1\n"
    "v_and_or_b32 v232, v232, s40, v224\n"
    "v_and_or_b32 v233, v233, s40, v224\n"
    "v_mfma_f32_16x16x32_bf16 v[160:163], v[24:27], v[180:183], v[160:163]\n"
    "ds_read_b128 v[176:179], v240 offset:24576\n"
    "v_and_or_b32 v234, v234, s40, v224\n"
    "v_and_or_b32 v235, v235, s40, v224\n"
    "v_mfma_f32_16x16x32_bf16 v[164:167], v[28:31], v[180:183], v[164:167]\n"
    "s_waitcnt lgkmcnt(3)\n"
    "v_mul_f32 v170, v202, v170\n"
    "v_mul_f32 v171, v203, v171\n"
    "v_mfma_f32_16x16x32_bf16 v[152:155], v[236:239], v[184:187], v[152:155]\n"
    "ds_read_b128 v[180:183], v245 offset:20480\n"
    "v_mul_f32 v172, v204, v172\n"
    "v_mul_f32 v173, v205, v173\n"
    "v_mfma_f32_16x16x32_bf16 v[156:159], v[236:239], v[188:191], v[156:159]\n"
    "s_waitcnt lgkmcnt(2)\n"
    "ds_read_b128 v[184:187], v240 offset:43008\n"
    "v_mul_f32 v174, v206, v174\n"
    "v_mul_f32 v175, v207, v175\n"
    "v_mfma_f32_16x16x32_bf16 v[160:163], v[32:35], v[192:195], v[160:163]\n"
    "ds_read_b128 v[188:191], v240 offset:61440\n"
    "v_pk_lshrrev_b16 v236, s41, v251 op_sel:[0,0] op_sel_hi:[1,0]\n"
    "v_pk_lshrrev_b16 v237, s42, v251 op_sel:[0,0] op_sel_hi:[1,0]\n"
    "v_mfma_f32_16x16x32_bf16 v[164:167], v[36:39], v[192:195], v[164:167]\n"
    "v_pk_lshrrev_b16 v238, s41, v251 op_sel:[0,1] op_sel_hi:[1,1]\n"
    "v_pk_lshrrev_b16 v239, s42, v251 op_sel:[0,1] op_sel_hi:[1,1]\n"
    "v_mfma_f32_16x16x32_bf16 v[144:147], v[232:235], v[196:199], v[144:147]\n"
    "s_waitcnt lgkmcnt(2)\n"
    "ds_read_b128 v[192:195], v244 offset:21504\n"
    "ds_read_b32 v250, v246 offset:8192\n"
    "v_and_or_b32 v236, v236, s40, v224\n"
    "v_and_or_b32 v237, v237, s40, v224\n"
    "v_mfma_f32_16x16x32_bf16 v[148:151], v[232:235], v[176:179], v[148:151]\n"
    "ds_read_b128 v[196:199], v240 offset:7168\n"
    "v_and_or_b32 v238, v238, s40, v224\n"
    "v_and_or_b32 v239, v239, s40, v224\n"
    "v_mfma_f32_16x16x32_bf16 v[160:163], v[40:43], v[180:183], v[160:163]\n"
    "ds_read_b128 v[176:179], v240 offset:25600\n"
    "v_add_f32 v230, v228, v229\n"
    "v_cvt_pk_bf16_f32 v168, v168, v172\n"
    "v_mfma_f32_16x16x32_bf16 v[164:167], v[44:47], v[180:183], v[164:167]\n"
    "s_waitcnt lgkmcnt(4)\n"
    "v_cvt_pk_bf16_f32 v169, v169, v173\n"
    "v_add_f32_dpp v231, v230, v230 row_ror:8 row_mask:0xf bank_mask:0xf\n"
    "v_mfma_f32_16x16x32_bf16 v[152:155], v[232:235], v[184:187], v[152:155]\n"
    "ds_read_b128 v[180:183], v245 offset:21504\n"
    "ds_write_b32 v254, v231 offset:256\n"
    "v_cvt_pk_bf16_f32 v170, v170, v174\n"
    "v_cvt_pk_bf16_f32 v171, v171, v175\n"
    "v_mfma_f32_16x16x32_bf16 v[156:159], v[232:235], v[188:191], v[156:159]\n"
    "s_waitcnt lgkmcnt(3)\n"
    "ds_read_b128 v[184:187], v240 offset:44032\n"
    "buffer_store_dwordx4 v[168:171], v248, s[36:39], s69 offen sc1\n"
    "v_mfma_f32_16x16x32_bf16 v[160:163], v[48:51], v[192:195], v[160:163]\n"
    "ds_read_b128 v[188:191], v240 offset:62464\n"
    "v_pk_lshrrev_b16 v232, s41, v250 op_sel:[0,0] op_sel_hi:[1,0]\n"
    "v_pk_lshrrev_b16 v233, s42, v250 op_sel:[0,0] op_sel_hi:[1,0]\n"
    "v_mfma_f32_16x16x32_bf16 v[164:167], v[52:55], v[192:195], v[164:167]\n"
    "v_pk_lshrrev_b16 v234, s41, v250 op_sel:[0,1] op_sel_hi:[1,1]\n"
    "v_pk_lshrrev_b16 v235, s42, v250 op_sel:[0,1] op_sel_hi:[1,1]\n"
    "v_mfma_f32_16x16x32_bf16 v[144:147], v[236:239], v[196:199], v[144:147]\n"
    "s_waitcnt lgkmcnt(3)\n"
    "ds_read_b128 v[192:195], v244 offset:22528\n"
    "v_and_or_b32 v232, v232, s40, v224\n"
    "v_and_or_b32 v233, v233, s40, v224\n"
    "v_mfma_f32_16x16x32_bf16 v[148:151], v[236:239], v[176:179], v[148:151]\n"
    "ds_read_b128 v[196:199], v240 offset:8192\n"
    "v_and_or_b32 v234, v234, s40, v224\n"
    "v_and_or_b32 v235, v235, s40, v224\n"
    "v_mfma_f32_16x16x32_bf16 v[160:163], v[56:59], v[180:183], v[160:163]\n"
    "ds_read_b128 v[176:179], v240 offset:26624\n"
    "s_setprio 0\n"
    "v_mfma_f32_16x16x32_bf16 v[164:167], v[60:63], v[180:183], v[164:167]\n"
    "s_waitcnt lgkmcnt(3)\n"
    "v_mfma_f32_16x16x32_bf16 v[152:155], v[236:239], v[184:187], v[152:155]\n"
    "ds_read_b128 v[180:183], v245 offset:22528\n"
    "v_mfma_f32_16x16x32_bf16 v[156:159], v[236:239], v[188:191], v[156:159]\n"
    "s_waitcnt lgkmcnt(2)\n"
    "ds_read_b128 v[184:187], v240 offset:45056\n"
    "v_mfma_f32_16x16x32_bf16 v[160:163], v[64:67], v[192:195], v[160:163]\n"
    "ds_read_b128 v[188:191], v240 offset:63488\n"
    "v_mfma_f32_16x16x32_bf16 v[164:167], v[68:71], v[192:195], v[164:167]\n"
    "v_mfma_f32_16x16x32_bf16 v[144:147], v[232:235], v[196:199], v[144:147]\n"
    "s_waitcnt lgkmcnt(2)\n"
    "ds_read_b128 v[192:195], v244 offset:23552\n"
    "v_add_u32 v255, s55, v252\n"
    "v_add_u32 v248, s76, v248\n"
    "v_mfma_f32_16x16x32_bf16 v[148:151], v[232:235], v[176:179], v[148:151]\n"
    "ds_read_b128 v[196:199], v245 offset:23552\n"
    "v_mfma_f32_16x16x32_bf16 v[160:163], v[72:75], v[180:183], v[160:163]\n"
    "ds_read_b128 v[176:179], v244 offset:24576\n"
    "v_mfma_f32_16x16x32_bf16 v[164:167], v[76:79], v[180:183], v[164:167]\n"
    "s_waitcnt lgkmcnt(3)\n"
    "v_mfma_f32_16x16x32_bf16 v[152:155], v[232:235], v[184:187], v[152:155]\n"
    "ds_read_b128 v[180:183], v245 offset:24576\n"
    "v_mfma_f32_16x16x32_bf16 v[156:159], v[232:235], v[188:191], v[156:159]\n"
    "s_waitcnt lgkmcnt(2)\n"
    "ds_read_b128 v[184:187], v244 offset:25600\n"
    "v_mfma_f32_16x16x32_bf16 v[160:163], v[80:83], v[192:195], v[160:163]\n"
    "ds_read_b128 v[188:191], v245 offset:25600\n"
    "s_setprio 3\n"
    "s_barrier\n"
    "ds_read_b32 v242, v249 offset:256\n"
    "ds_read_b32 v243, v249 offset:320\n"
    "ds_read_b32 v241, v249 offset:448\n"
    "v_mfma_f32_16x16x32_bf16 v[164:167], v[84:87], v[192:195], v[164:167]\n"
    "v_mfma_f32_16x16x32_bf16 v[160:163], v[88:91], v[196:199], v[160:163]\n"
    "ds_read_b128 v[192:195], v244 offset:26624\n"
    "ds_write_b128 v255, v[144:147] offset:0\n"
    "s_waitcnt lgkmcnt(4)\n"
    "v_fmac_f32 v216, v208, v242\n"
    "v_fmac_f32 v217, v209, v242\n"
    "v_mfma_f32_16x16x32_bf16 v[164:167], v[92:95], v[196:199], v[164:167]\n"
    "v_fmac_f32 v218, v210, v242\n"
    "v_fmac_f32 v219, v211, v242\n"
    "v_mfma_f32_16x16x32_bf16 v[160:163], v[96:99], v[176:179], v[160:163]\n"
    "ds_read_b128 v[196:199], v245 offset:26624\n"
    "s_setprio 2\n"
    "v_fmac_f32 v220, v212, v242\n"
    "v_mfma_f32_16x16x32_bf16 v[164:167], v[100:103], v[176:179], v[164:167]\n"
    "v_mfma_f32_16x16x32_bf16 v[160:163], v[104:107], v[180:183], v[160:163]\n"
    "ds_read_b128 v[176:179], v244 offset:36864\n"
    "ds_write_b128 v255, v[148:151] offset:4096\n"
    "v_fmac_f32 v221, v213, v242\n"
    "v_fmac_f32 v222, v214, v242\n"
    "v_mfma_f32_16x16x32_bf16 v[164:167], v[108:111], v[180:183], v[164:167]\n"
    "v_fmac_f32 v223, v215, v242\n"
    "v_mul_f32 v216, v200, v216\n"
    "v_mfma_f32_16x16x32_bf16 v[160:163], v[112:115], v[184:187], v[160:163]\n"
    "ds_read_b128 v[180:183], v245 offset:36864\n"
    "v_mul_f32 v217, v201, v217\n"
    "v_mfma_f32_16x16x32_bf16 v[164:167], v[116:119], v[184:187], v[164:167]\n"
    "v_mfma_f32_16x16x32_bf16 v[160:163], v[120:123], v[188:191], v[160:163]\n"
    "ds_read_b128 v[184:187], v244 offset:37888\n"
    "s_setprio 1\n"
    "ds_write_b128 v255, v[152:155] offset:8192\n"
    "v_mul_f32 v218, v202, v218\n"
    "v_mul_f32 v219, v203, v219\n"
    "v_mfma_f32_16x16x32_bf16 v[164:167], v[124:127], v[188:191], v[164:167]\n"
    "s_waitcnt lgkmcnt(5)\n"
    "v_mul_f32 v220, v204, v220\n"
    "v_mul_f32 v221, v205, v221\n"
    "v_mfma_f32_16x16x32_bf16 v[160:163], v[128:131], v[192:195], v[160:163]\n"
    "ds_read_b128 v[188:191], v245 offset:37888\n"
    "v_mul_f32 v222, v206, v222\n"
    "v_mfma_f32_16x16x32_bf16 v[164:167], v[132:135], v[192:195], v[164:167]\n"
    "v_mfma_f32_16x16x32_bf16 v[160:163], v[136:139], v[196:199], v[160:163]\n"
    "ds_read_b128 v[192:195], v244 offset:38912\n"
    "ds_write_b128 v255, v[156:159] offset:12288\n"
    "v_mul_f32 v223, v207, v223\n"
    "v_cvt_pk_bf16_f32 v216, v216, v220\n"
    "v_mfma_f32_16x16x32_bf16 v[164:167], v[140:143], v[196:199], v[164:167]\n"
    "s_waitcnt lgkmcnt(5)\n"
    "v_cvt_pk_bf16_f32 v217, v217, v221\n"
    "v_cvt_pk_bf16_f32 v218, v218, v222\n"
    "v_mfma_f32_16x16x32_bf16 v[168:171], v[0:3], v[176:179], 0\n"
    "ds_read_b128 v[196:199], v245 offset:38912\n"
    "s_setprio 0\n"
    "v_cvt_pk_bf16_f32 v219, v219, v223\n"
    "v_mfma_f32_16x16x32_bf16 v[172:175], v[4:7], v[176:179], 0\n"
    "buffer_store_dwordx4 v[216:219], v248, s[36:39], 0 offen sc1\n"
    "v_add_u32 v255, s56, v252\n"
    "v_mfma_f32_16x16x32_bf16 v[168:171], v[8:11], v[180:183], v[168:171]\n"
    "ds_read_b128 v[176:179], v244 offset:39936\n"
    "s_lshl_b32 s44, %2, 6\n"
    "v_add_u32 v254, s44, v249\n"
    "v_mfma_f32_16x16x32_bf16 v[172:175], v[12:15], v[180:183], v[172:175]\n"
    "s_waitcnt lgkmcnt(4)\n"
    "ds_read_b128 v[232:235], v253 offset:2432\n"
    "ds_read_b128 v[236:239], v253 offset:2448\n"
    "ds_read_b32 v242, v254\n"
    "v_mul_f32 v220, v212, v241\n"
    "v_mul_f32 v221, v213, v241\n"
    "v_mfma_f32_16x16x32_bf16 v[168:171], v[16:19], v[184:187], v[168:171]\n"
    "ds_read_b128 v[180:183], v245 offset:39936\n"
    "v_mul_f32 v222, v214, v241\n"
    "v_mul_f32 v223, v215, v241\n"
    "v_mfma_f32_16x16x32_bf16 v[172:175], v[20:23], v[184:187], v[172:175]\n"
    "v_mul_f32 v216, v208, v241\n"
    "v_mul_f32 v217, v209, v241\n"
    "v_mfma_f32_16x16x32_bf16 v[168:171], v[24:27], v[188:191], v[168:171]\n"
    "ds_read_b128 v[184:187], v244 offset:40960\n"
    "s_setprio 3\n"
    "v_mul_f32 v218, v210, v241\n"
    "v_mul_f32 v219, v211, v241\n"
    "v_mfma_f32_16x16x32_bf16 v[172:175], v[28:31], v[188:191], v[172:175]\n"
    "s_waitcnt lgkmcnt(6)\n"
    "v_fmac_f32 v160, v208, v243\n"
    "v_fmac_f32 v161, v209, v243\n"
    "v_mfma_f32_16x16x32_bf16 v[168:171], v[32:35], v[192:195], v[168:171]\n"
    "ds_read_b128 v[188:191], v245 offset:40960\n"
    "v_fmac_f32 v162, v210, v243\n"
    "v_fmac_f32 v163, v211, v243\n"
    "v_mfma_f32_16x16x32_bf16 v[172:175], v[36:39], v[192:195], v[172:175]\n"
    "v_fmac_f32 v164, v212, v243\n"
    "v_fmac_f32 v165, v213, v243\n"
    "v_mfma_f32_16x16x32_bf16 v[168:171], v[40:43], v[196:199], v[168:171]\n"
    "ds_read_b128 v[192:195], v244 offset:41984\n"
    "v_fmac_f32 v166, v214, v243\n"
    "v_fmac_f32 v167, v215, v243\n"
    "v_mfma_f32_16x16x32_bf16 v[172:175], v[44:47], v[196:199], v[172:175]\n"
    "s_waitcnt lgkmcnt(3)\n"
    "s_barrier\n"
    "ds_read_b128 v[144:147], v255 offset:0\n"
    "v_mul_f32 v160, v200, v160\n"
    "v_mul_f32 v161, v201, v161\n"
    "v_mfma_f32_16x16x32_bf16 v[168:171], v[48:51], v[176:179], v[168:171]\n"
    "ds_read_b128 v[196:199], v245 offset:41984\n"
    "ds_read_b128 v[152:155], v255 offset:2048\n"
    "v_mul_f32 v162, v202, v162\n"
    "v_mul_f32 v163, v203, v163\n"
    "v_mfma_f32_16x16x32_bf16 v[172:175], v[52:55], v[176:179], v[172:175]\n"
    "ds_read_b128 v[148:151], v255 offset:1024\n"
    "v_mul_f32 v164, v204, v164\n"
    "v_mul_f32 v165, v205, v165\n"
    "v_mfma_f32_16x16x32_bf16 v[168:171], v[56:59], v[180:183], v[168:171]\n"
    "ds_read_b128 v[176:179], v244 offset:43008\n"
    "ds_read_b128 v[156:159], v255 offset:3072\n"
    "v_mul_f32 v166, v206, v166\n"
    "v_mul_f32 v167, v207, v167\n"
    "v_mfma_f32_16x16x32_bf16 v[172:175], v[60:63], v[180:183], v[172:175]\n"
    "s_waitcnt lgkmcnt(7)\n"
    "ds_read_b128 v[224:227], v253 offset:1024\n"
    "v_cvt_pk_bf16_f32 v160, v160, v164\n"
    "v_cvt_pk_bf16_f32 v161, v161, v165\n"
    "v_mfma_f32_16x16x32_bf16 v[168:171], v[64:67], v[184:187], v[168:171]\n"
    "ds_read_b128 v[180:183], v245 offset:43008\n"
    "ds_read_b128 v[228:231], v253 offset:1040\n"
    "v_cvt_pk_bf16_f32 v162, v162, v166\n"
    "v_cvt_pk_bf16_f32 v163, v163, v167\n"
    "v_mfma_f32_16x16x32_bf16 v[172:175], v[68:71], v[184:187], v[172:175]\n"
    "buffer_store_dwordx4 v[160:163], v248, s[36:39], s65 offen sc1\n"
    "ds_read_b32 v243, v249 offset:384\n"
    "s_waitcnt lgkmcnt(7)\n"
    "v_add_f32 v144, v144, v152\n"
    "v_add_f32 v145, v145, v153\n"
    "v_mfma_f32_16x16x32_bf16 v[168:171], v[72:75], v[188:191], v[168:171]\n"
    "ds_read_b128 v[184:187], v244 offset:44032\n"
    "v_add_f32 v146, v146, v154\n"
    "v_add_f32 v147, v147, v155\n"
    "v_mfma_f32_16x16x32_bf16 v[172:175], v[76:79], v[188:191], v[172:175]\n"
    "s_waitcnt lgkmcnt(5)\n"
    "v_add_f32 v148, v148, v156\n"
    "v_add_f32 v149, v149, v157\n"
    "v_mfma_f32_16x16x32_bf16 v[168:171], v[80:83], v[192:195], v[168:171]\n"
    "ds_read_b128 v[188:191], v245 offset:44032\n"
    "v_add_f32 v150, v150, v158\n"
    "v_add_f32 v151, v151, v159\n"
    "v_mfma_f32_16x16x32_bf16 v[172:175], v[84:87], v[192:195], v[172:175]\n"
    "v_fmac_f32 v144, v232, v242\n"
    "v_fmac_f32 v145, v233, v242\n"
    "v_mfma_f32_16x16x32_bf16 v[168:171], v[88:91], v[196:199], v[168:171]\n"
    "ds_read_b128 v[192:195], v244 offset:45056\n"
    "s_setprio 2\n"
    "v_fmac_f32 v146, v234, v242\n"
    "v_fmac_f32 v147, v235, v242\n"
    "v_mfma_f32_16x16x32_bf16 v[172:175], v[92:95], v[196:199], v[172:175]\n"
    "s_waitcnt lgkmcnt(5)\n"
    "v_fmac_f32 v148, v236, v242\n"
    "v_fmac_f32 v149, v237, v242\n"
    "v_mfma_f32_16x16x32_bf16 v[168:171], v[96:99], v[176:179], v[168:171]\n"
    "ds_read_b128 v[196:199], v245 offset:45056\n"
    "v_fmac_f32 v150, v238, v242\n"
    "v_fmac_f32 v151, v239, v242\n"
    "v_mfma_f32_16x16x32_bf16 v[172:175], v[100:103], v[176:179], v[172:175]\n"
    "v_add_u32 v255, s51, v248\n"
    "v_mul_f32 v144, v224, v144\n"
    "v_mfma_f32_16x16x32_bf16 v[168:171], v[104:107], v[180:183], v[168:171]\n"
    "ds_read_b128 v[176:179], v244 offset:55296\n"
    "v_mul_f32 v145, v225, v145\n"
    "v_mul_f32 v146, v226, v146\n"
    "v_mfma_f32_16x16x32_bf16 v[172:175], v[108:111], v[180:183], v[172:175]\n"
    "s_waitcnt lgkmcnt(3)\n"
    "v_mul_f32 v147, v227, v147\n"
    "v_mul_f32 v148, v228, v148\n"
    "v_mfma_f32_16x16x32_bf16 v[168:171], v[112:115], v[184:187], v[168:171]\n"
    "ds_read_b128 v[180:183], v245 offset:55296\n"
    "v_mul_f32 v149, v229, v149\n"
    "v_mul_f32 v150, v230, v150\n"
    "v_mfma_f32_16x16x32_bf16 v[172:175], v[116:119], v[184:187], v[172:175]\n"
    "v_mul_f32 v151, v231, v151\n"
    "v_cvt_pk_bf16_f32 v144, v144, v148\n"
    "v_mfma_f32_16x16x32_bf16 v[168:171], v[120:123], v[188:191], v[168:171]\n"
    "ds_read_b128 v[184:187], v244 offset:56320\n"
    "v_cvt_pk_bf16_f32 v145, v145, v149\n"
    "v_cvt_pk_bf16_f32 v146, v146, v150\n"
    "v_mfma_f32_16x16x32_bf16 v[172:175], v[124:127], v[188:191], v[172:175]\n"
    "s_waitcnt lgkmcnt(3)\n"
    "v_cvt_pk_bf16_f32 v147, v147, v151\n"
    "v_mfma_f32_16x16x32_bf16 v[168:171], v[128:131], v[192:195], v[168:171]\n"
    "ds_read_b128 v[188:191], v245 offset:56320\n"
    "buffer_store_dwordx4 v[144:147], v255, s[36:39], 0 offen sc1\n"
    "v_mfma_f32_16x16x32_bf16 v[172:175], v[132:135], v[192:195], v[172:175]\n"
    "v_mfma_f32_16x16x32_bf16 v[168:171], v[136:139], v[196:199], v[168:171]\n"
    "ds_read_b128 v[192:195], v244 offset:57344\n"
    "v_mfma_f32_16x16x32_bf16 v[172:175], v[140:143], v[196:199], v[172:175]\n"
    "s_waitcnt lgkmcnt(3)\n"
    "v_mfma_f32_16x16x32_bf16 v[216:219], v[0:3], v[176:179], v[216:219]\n"
    "ds_read_b128 v[196:199], v245 offset:57344\n"
    "v_mfma_f32_16x16x32_bf16 v[220:223], v[4:7], v[176:179], v[220:223]\n"
    "v_mfma_f32_16x16x32_bf16 v[216:219], v[8:11], v[180:183], v[216:219]\n"
    "ds_read_b128 v[176:179], v244 offset:58368\n"
    "s_setprio 1\n"
    "v_fmac_f32 v168, v208, v243\n"
    "v_fmac_f32 v169, v209, v243\n"
    "v_mfma_f32_16x16x32_bf16 v[220:223], v[12:15], v[180:183], v[220:223]\n"
    "s_waitcnt lgkmcnt(3)\n"
    "v_fmac_f32 v170, v210, v243\n"
    "v_fmac_f32 v171, v211, v243\n"
    "v_mfma_f32_16x16x32_bf16 v[216:219], v[16:19], v[184:187], v[216:219]\n"
    "ds_read_b128 v[180:183], v245 offset:58368\n"
    "v_fmac_f32 v172, v212, v243\n"
    "v_fmac_f32 v173, v213, v243\n"
    "v_mfma_f32_16x16x32_bf16 v[220:223], v[20:23], v[184:187], v[220:223]\n"
    "v_fmac_f32 v174, v214, v243\n"
    "v_fmac_f32 v175, v215, v243\n"
    "v_mfma_f32_16x16x32_bf16 v[216:219], v[24:27], v[188:191], v[216:219]\n"
    "ds_read_b128 v[184:187], v244 offset:59392\n"
    "v_mul_f32 v168, v200, v168\n"
    "v_mul_f32 v169, v201, v169\n"
    "v_mfma_f32_16x16x32_bf16 v[220:223], v[28:31], v[188:191], v[220:223]\n"
    "s_waitcnt lgkmcnt(3)\n"
    "v_mul_f32 v170, v202, v170\n"
    "v_mul_f32 v171, v203, v171\n"
    "v_mfma_f32_16x16x32_bf16 v[216:219], v[32:35], v[192:195], v[216:219]\n"
    "ds_read_b128 v[188:191], v245 offset:59392\n"
    "v_mul_f32 v172, v204, v172\n"
    "v_mul_f32 v173, v205, v173\n"
    "v_mfma_f32_16x16x32_bf16 v[220:223], v[36:39], v[192:195], v[220:223]\n"
    "v_mul_f32 v174, v206, v174\n"
    "v_mul_f32 v175, v207, v175\n"
    "v_mfma_f32_16x16x32_bf16 v[216:219], v[40:43], v[196:199], v[216:219]\n"
    "ds_read_b128 v[192:195], v244 offset:60416\n"
    "v_cvt_pk_bf16_f32 v168, v168, v172\n"
    "v_cvt_pk_bf16_f32 v169, v169, v173\n"
    "v_mfma_f32_16x16x32_bf16 v[220:223], v[44:47], v[196:199], v[220:223]\n"
    "s_waitcnt lgkmcnt(3)\n"
    "v_cvt_pk_bf16_f32 v170, v170, v174\n"
    "v_cvt_pk_bf16_f32 v171, v171, v175\n"
    "v_mfma_f32_16x16x32_bf16 v[216:219], v[48:51], v[176:179], v[216:219]\n"
    "ds_read_b128 v[196:199], v245 offset:60416\n"
    "buffer_store_dwordx4 v[168:171], v248, s[36:39], s67 offen sc1\n"
    "v_mfma_f32_16x16x32_bf16 v[220:223], v[52:55], v[176:179], v[220:223]\n"
    "v_mfma_f32_16x16x32_bf16 v[216:219], v[56:59], v[180:183], v[216:219]\n"
    "ds_read_b128 v[176:179], v244 offset:61440\n"
    "v_mfma_f32_16x16x32_bf16 v[220:223], v[60:63], v[180:183], v[220:223]\n"
    "s_waitcnt lgkmcnt(3)\n"
    "v_mfma_f32_16x16x32_bf16 v[216:219], v[64:67], v[184:187], v[216:219]\n"
    "ds_read_b128 v[180:183], v245 offset:61440\n"
    "v_mfma_f32_16x16x32_bf16 v[220:223], v[68:71], v[184:187], v[220:223]\n"
    "v_mfma_f32_16x16x32_bf16 v[216:219], v[72:75], v[188:191], v[216:219]\n"
    "ds_read_b128 v[184:187], v244 offset:62464\n"
    "s_setprio 0\n"
    "v_mfma_f32_16x16x32_bf16 v[220:223], v[76:79], v[188:191], v[220:223]\n"
    "s_waitcnt lgkmcnt(3)\n"
    "v_mfma_f32_16x16x32_bf16 v[216:219], v[80:83], v[192:195], v[216:219]\n"
    "ds_read_b128 v[188:191], v245 offset:62464\n"
    "v_mfma_f32_16x16x32_bf16 v[220:223], v[84:87], v[192:195], v[220:223]\n"
    "v_mfma_f32_16x16x32_bf16 v[216:219], v[88:91], v[196:199], v[216:219]\n"
    "ds_read_b128 v[192:195], v244 offset:63488\n"
    "v_mfma_f32_16x16x32_bf16 v[220:223], v[92:95], v[196:199], v[220:223]\n"
    "s_waitcnt lgkmcnt(3)\n"
    "v_mfma_f32_16x16x32_bf16 v[216:219], v[96:99], v[176:179], v[216:219]\n"
    "ds_read_b128 v[196:199], v245 offset:63488\n"
    "v_mfma_f32_16x16x32_bf16 v[220:223], v[100:103], v[176:179], v[220:223]\n"
    "v_mfma_f32_16x16x32_bf16 v[216:219], v[104:107], v[180:183], v[216:219]\n"
    "v_mfma_f32_16x16x32_bf16 v[220:223], v[108:111], v[180:183], v[220:223]\n"
    "s_waitcnt lgkmcnt(2)\n"
    "v_mfma_f32_16x16x32_bf16 v[216:219], v[112:115], v[184:187], v[216:219]\n"
    "v_mfma_f32_16x16x32_bf16 v[220:223], v[116:119], v[184:187], v[220:223]\n"
    "v_mfma_f32_16x16x32_bf16 v[216:219], v[120:123], v[188:191], v[216:219]\n"
    "v_mfma_f32_16x16x32_bf16 v[220:223], v[124:127], v[188:191], v[220:223]\n"
    "s_waitcnt lgkmcnt(0)\n"
    "v_mfma_f32_16x16x32_bf16 v[216:219], v[128:131], v[192:195], v[216:219]\n"
    "v_mfma_f32_16x16x32_bf16 v[220:223], v[132:135], v[192:195], v[220:223]\n"
    "v_mfma_f32_16x16x32_bf16 v[216:219], v[136:139], v[196:199], v[216:219]\n"
    "v_mfma_f32_16x16x32_bf16 v[220:223], v[140:143], v[196:199], v[220:223]\n"
    "s_nop 6\n"
    "v_mul_f32 v216, v200, v216\n"
    "v_mul_f32 v217, v201, v217\n"
    "v_mul_f32 v218, v202, v218\n"
    "v_mul_f32 v219, v203, v219\n"
    "v_mul_f32 v220, v204, v220\n"
    "v_mul_f32 v221, v205, v221\n"
    "v_mul_f32 v222, v206, v222\n"
    "v_mul_f32 v223, v207, v223\n"
    "v_cvt_pk_bf16_f32 v216, v216, v220\n"
    "v_cvt_pk_bf16_f32 v217, v217, v221\n"
    "v_cvt_pk_bf16_f32 v218, v218, v222\n"
    "v_cvt_pk_bf16_f32 v219, v219, v223\n"
    "buffer_store_dwordx4 v[216:219], v248, s[36:39], s69 offen sc1\n"
    "s_endpgm\n.fill 512, 4, 0xbf800000\n.Lend_%=:\n"
  :
  : "s"(ka), "s"(bid), "s"(wave), "s"(lbase)
  : "memory", "scc", "vcc", "v0", "v1", "v2", "v3", "v4", "v5", "v6", "v7", "v8", "v9", "v10", "v11", "v12", "v13", "v14", "v15", "v16", "v17", "v18", "v19", "v20", "v21", "v22", "v23", "v24", "v25", "v26", "v27", "v28", "v29", "v30", "v31", "v32", "v33", "v34", "v35", "v36", "v37", "v38", "v39", "v40", "v41", "v42", "v43", "v44", "v45", "v46", "v47", "v48", "v49", "v50", "v51", "v52", "v53", "v54", "v55", "v56", "v57", "v58", "v59", "v60", "v61", "v62", "v63", "v64", "v65", "v66", "v67", "v68", "v69", "v70", "v71", "v72", "v73", "v74", "v75", "v76", "v77", "v78", "v79", "v80", "v81", "v82", "v83", "v84", "v85", "v86", "v87", "v88", "v89", "v90", "v91", "v92", "v93", "v94", "v95", "v96", "v97", "v98", "v99", "v100", "v101", "v102", "v103", "v104", "v105", "v106", "v107", "v108", "v109", "v110", "v111", "v112", "v113", "v114", "v115", "v116", "v117", "v118", "v119", "v120", "v121", "v122", "v123", "v124", "v125", "v126", "v127", "v128", "v129", "v130", "v131", "v132", "v133", "v134", "v135", "v136", "v137", "v138", "v139", "v140", "v141", "v142", "v143", "v144", "v145", "v146", "v147", "v148", "v149", "v150", "v151", "v152", "v153", "v154", "v155", "v156", "v157", "v158", "v159", "v160", "v161", "v162", "v163", "v164", "v165", "v166", "v167", "v168", "v169", "v170", "v171", "v172", "v173", "v174", "v175", "v176", "v177", "v178", "v179", "v180", "v181", "v182", "v183", "v184", "v185", "v186", "v187", "v188", "v189", "v190", "v191", "v192", "v193", "v194", "v195", "v196", "v197", "v198", "v199", "v200", "v201", "v202", "v203", "v204", "v205", "v206", "v207", "v208", "v209", "v210", "v211", "v212", "v213", "v214", "v215", "v216", "v217", "v218", "v219", "v220", "v221", "v222", "v223", "v224", "v225", "v226", "v227", "v228", "v229", "v230", "v231", "v232", "v233", "v234", "v235", "v236", "v237", "v238", "v239", "v240", "v241", "v242", "v243", "v244", "v245", "v246", "v247", "v248", "v249", "v250", "v251", "v252", "v253", "v254", "v255", "s12", "s13", "s14", "s15", "s16", "s17", "s18", "s19", "s20", "s21", "s22", "s23", "s24", "s25", "s26", "s27", "s28", "s29", "s30", "s31", "s36", "s37", "s38", "s39", "s40", "s41", "s42", "s43", "s44", "s45", "s46", "s47", "s48", "s49", "s50", "s51", "s52", "s53", "s54", "s55", "s56", "s57", "s58", "s59", "s60", "s61", "s62", "s63", "s64", "s65", "s66", "s67", "s68", "s69", "s70", "s71", "s72", "s73", "s74", "s75", "s76", "s77", "s78", "s79");
}

extern "C" void kernel_launch(void* const* d_in, const int* in_sizes, int n_in, void* d_out, int out_size, void* d_ws,
                              size_t ws_size, hipStream_t stream) {
  gptq_gemm<<<256, 512, 0, stream>>>(d_in[0], d_in[1], d_in[2], d_in[3], d_out);
}
```
